```python
import torch

def _softmax(x, axis=-1):
    return torch.softmax(x, dim=axis)
def _gelu(x):
    return torch.nn.functional.gelu(x, approximate="tanh")
def _silu(x):
    return torch.nn.functional.silu(x)
def _sigmoid(x):
    return torch.sigmoid(x)
def _layernorm(x, axis=-1, eps=1e-5):
    m = x.mean(dim=axis, keepdim=True)
    v = x.var(dim=axis, keepdim=True, unbiased=False)
    return (x - m) / torch.sqrt(v + eps)
def _scatter_add(tgt, idx, src, axis=0):
    return tgt.clone().index_add_(axis, idx, src)
def _segment_sum(src, seg, n):
    out = torch.zeros((n,) + src.shape[1:], dtype=src.dtype)
    return out.index_add_(0, seg, src)
def _pad(x, pw):
    flat = tuple(v for pair in reversed(pw) for v in pair)
    return torch.nn.functional.pad(x, flat)

kernel_name = 'sequential_preLN__mha_block_cached__glu_gated_geglu__decode'

def setup_inputs():

    _w = (lambda a: (lambda t: t.to(torch.bfloat16) if t.is_floating_point() else t)(torch.as_tensor(a)))
    def _A_setup():
        B, Sq, D = 2, 1, 576
        H, Sk, Dh = 32, 8192, 128
        return {
            "x": _w((lambda *s: torch.randn(*s, dtype=torch.bfloat16) * 0.05)(B, Sq, D)),
            "k_cache": _w((lambda *s: torch.randn(*s, dtype=torch.bfloat16) * 1.6)(B, H, Sk, Dh)),
            "v_cache": _w((lambda *s: torch.randn(*s, dtype=torch.bfloat16) * 0.8)(B, H, Sk, Dh)),
            "wq": _w((lambda *s: torch.randn(*s, dtype=torch.bfloat16) * 1.6)(D, H * Dh) * (D ** -0.5)),
            "wo": _w((lambda *s: torch.randn(*s, dtype=torch.bfloat16) * 0.8)(H * Dh, D) * ((H * Dh) ** -0.5)),
        }
    _A_in = _A_setup()

    def _B_setup():
        B, S, Dm, Df = 2, 1, 576, 4608
        return {
            "x": _w((lambda *s: torch.randn(*s, dtype=torch.bfloat16) * 0.05)(B, S, Dm)),
            "w_gate": _w((lambda *s: torch.randn(*s, dtype=torch.bfloat16) * 0.8)(Dm, Df) * (Dm ** -0.5)),
            "w_up": _w((lambda *s: torch.randn(*s, dtype=torch.bfloat16) * 0.8)(Dm, Df) * (Dm ** -0.5)),
            "w_down": _w((lambda *s: torch.randn(*s, dtype=torch.bfloat16) * 0.8)(Df, Dm) * (Df ** -0.5)),
        }
    _B_in = _B_setup()

    return {
        "A_x": _A_in["x"],
        "A_k_cache": _A_in["k_cache"],
        "A_v_cache": _A_in["v_cache"],
        "A_wq": _A_in["wq"],
        "A_wo": _A_in["wo"],
        "B_x": _B_in["x"],
        "B_w_gate": _B_in["w_gate"],
        "B_w_up": _B_in["w_up"],
        "B_w_down": _B_in["w_down"],
    }

def reference(A_x, A_k_cache, A_v_cache, A_wq, A_wo, B_x, B_w_gate, B_w_up, B_w_down):
    def _A_ref(x, k_cache, v_cache, wq, wo):
        B, Sq, D = x.shape
        H, Dh = 32, 128
        q = (lambda x, *d: x.permute(*d))((x @ wq).reshape(B, Sq, H, Dh), 0, 2, 1, 3)
        scores = torch.einsum("bhqd,bhkd->bhqk", q, k_cache) * (Dh ** -0.5)
        p = _softmax(scores, axis=-1)
        o = torch.einsum("bhqk,bhkd->bhqd", p, v_cache)
        return (lambda x, *d: x.permute(*d))(o, 0, 2, 1, 3).reshape(B, Sq, H * Dh) @ wo

    def _B_ref(x, w_gate, w_up, w_down):
        g = torch.einsum("bsd,df->bsf", x, w_gate)
        u = torch.einsum("bsd,df->bsf", x, w_up)
        h = _gelu(g) * u
        return torch.einsum("bsf,fd->bsd", h, w_down)

    x = A_x
    h = x + _A_ref(_layernorm(x, axis=-1), A_k_cache, A_v_cache, A_wq, A_wo)
    h2 = h + _B_ref(_layernorm(h, axis=-1), B_w_gate, B_w_up, B_w_down)
    return h2
```

```cpp
#include <hip/hip_runtime.h>
#include <hip/hip_bf16.h>
#include <stdint.h>

typedef unsigned short u16;
typedef unsigned v4u __attribute__((ext_vector_type(4)));
typedef __bf16 bf16x2 __attribute__((ext_vector_type(2)));
constexpr int B = 2, D = 576, H = 32, SK = 8192, DH = 128, HD = H * DH, DF = 4608;
constexpr int NSPLIT = 4;

__device__ __forceinline__ float bf2f(u16 b) { return __uint_as_float((unsigned)b << 16); }
__device__ __forceinline__ float rbf(float f) {
  unsigned u = __float_as_uint(f);
  u += 0x7FFFu + ((u >> 16) & 1u);
  return __uint_as_float(u & 0xFFFF0000u);
}
__device__ __forceinline__ u16 f2bf(float f) { return (u16)(__float_as_uint(rbf(f)) >> 16); }
__device__ __forceinline__ float dot2(unsigned a, unsigned b, float c) {
  return __builtin_amdgcn_fdot2_f32_bf16(__builtin_bit_cast(bf16x2, a), __builtin_bit_cast(bf16x2, b), c, false);
}
template <int CTRL> __device__ __forceinline__ float dpp(float x) {
  return __builtin_bit_cast(float, __builtin_amdgcn_mov_dpp(__builtin_bit_cast(int, x), CTRL, 0xf, 0xf, true));
}
__device__ __forceinline__ float wave_sum(float v) {
  for (int m = 32; m >= 1; m >>= 1) v += __shfl_xor(v, m);
  return v;
}
__device__ void ln_wave(const float* src, float* dst, int lane) {
  float v[9], s = 0.f;
  for (int i = 0; i < 9; ++i) { v[i] = src[lane + 64 * i]; s += v[i]; }
  s = wave_sum(s);
  const float mean = s * (1.f / D);
  float q = 0.f;
  for (int i = 0; i < 9; ++i) { float d = v[i] - mean; q += d * d; }
  q = wave_sum(q);
  const float mb = rbf(mean), vb = rbf(q * (1.f / D));
  const float den = rbf(sqrtf(rbf(vb + 1e-5f)));
  for (int i = 0; i < 9; ++i) dst[lane + 64 * i] = rbf(rbf(v[i] - mb) / den);
}

__global__ void __launch_bounds__(256) k_q(const u16* x, const u16* wq, u16* q) {
  __shared__ float xs[2][D], ln[2][D], red[4][2][64];
  const int tid = threadIdx.x, lane = tid & 63, w = tid >> 6;
  for (int i = tid; i < 2 * D; i += 256) xs[i / D][i % D] = bf2f(x[i]);
  __syncthreads();
  if (w < 2) ln_wave(xs[w], ln[w], lane);
  __syncthreads();
  const int n = blockIdx.x * 64 + lane;
  float a0 = 0.f, a1 = 0.f;
  for (int k = w; k < D; k += 4) { float wv = bf2f(wq[(size_t)k * HD + n]); a0 += ln[0][k] * wv; a1 += ln[1][k] * wv; }
  red[w][0][lane] = a0; red[w][1][lane] = a1;
  __syncthreads();
  if (w < 2) q[w * HD + n] = f2bf(red[0][w][lane] + red[1][w][lane] + red[2][w][lane] + red[3][w][lane]);
}

__global__ void __launch_bounds__(512) k_attn(const u16* q, const u16* K, const u16* V, float* part) {
  __shared__ float mrg[32][132];
  const int tid = threadIdx.x, lane = tid & 63, w = __builtin_amdgcn_readfirstlane(tid >> 6), r = lane >> 4, c = lane & 15;
  const int bh = blockIdx.x >> 2, s = blockIdx.x & 3;
  const v4u qv = *(const v4u*)(q + bh * DH + 8 * c);
  const size_t base = ((size_t)bh * SK + s * (SK / NSPLIT)) * DH;
  const v4u* kp = (const v4u*)(K + base) + lane;
  const v4u* vp = (const v4u*)(V + base) + lane;
  float m = -INFINITY, l = 0.f, acc[8];
  for (int i = 0; i < 8; ++i) acc[i] = 0.f;
  constexpr float L2E = 1.4426950408889634f;
  for (int t = 0; t < 16; ++t) {
    const int o = (t * 8 + w) * 256;
    v4u kk[4], vv[4];
#pragma unroll
    for (int i = 0; i < 4; ++i) { kk[i] = __builtin_nontemporal_load(kp + o + 64 * i); vv[i] = __builtin_nontemporal_load(vp + o + 64 * i); }
    float sc[4];
#pragma unroll
    for (int i = 0; i < 4; ++i) {
      float d = dot2(kk[i].x, qv.x, 0.f); d = dot2(kk[i].y, qv.y, d); d = dot2(kk[i].z, qv.z, d); d = dot2(kk[i].w, qv.w, d);
      d += dpp<0x128>(d); d += dpp<0x124>(d); d += dpp<0x122>(d); d += dpp<0x121>(d);
      sc[i] = rbf(rbf(d) * 0.08838834764831845f) * L2E;
    }
    const float mn = fmaxf(fmaxf(m, fmaxf(sc[0], sc[1])), fmaxf(sc[2], sc[3]));
    const float al = __builtin_amdgcn_exp2f(m - mn);
    m = mn; l *= al;
#pragma unroll
    for (int j = 0; j < 8; ++j) acc[j] *= al;
#pragma unroll
    for (int i = 0; i < 4; ++i) {
      const float p = __builtin_amdgcn_exp2f(sc[i] - mn);
      l += p;
#pragma unroll
      for (int j = 0; j < 4; ++j) {
        acc[2 * j] = fmaf(p, __uint_as_float(vv[i][j] << 16), acc[2 * j]);
        acc[2 * j + 1] = fmaf(p, __uint_as_float(vv[i][j] & 0xFFFF0000u), acc[2 * j + 1]);
      }
    }
  }
  float* rec = mrg[w * 4 + r];
  for (int j = 0; j < 8; ++j) rec[4 + 8 * c + j] = acc[j];
  if (c == 0) { rec[0] = m; rec[1] = l; }
  __syncthreads();
  if (tid < 128) {
    float M = -INFINITY;
    for (int i = 0; i < 32; ++i) M = fmaxf(M, mrg[i][0]);
    float L = 0.f, o = 0.f;
    for (int i = 0; i < 32; ++i) { const float wt = __builtin_amdgcn_exp2f(mrg[i][0] - M); L += wt * mrg[i][1]; o += wt * mrg[i][4 + tid]; }
    float* pp = part + (size_t)blockIdx.x * 132;
    pp[4 + tid] = o;
    if (tid == 0) { pp[0] = M; pp[1] = L; }
  }
}

__global__ void __launch_bounds__(576) k_wo(const float* part, const u16* wo, float* yp) {
  __shared__ float os[2][DH];
  const int tid = threadIdx.x, h = blockIdx.x;
  if (tid < 256) {
    const int b = tid >> 7, d = tid & 127;
    const float* pp = part + (size_t)((b * H + h) * 4) * 132;
    float M = -INFINITY;
    for (int s = 0; s < 4; ++s) M = fmaxf(M, pp[s * 132]);
    float L = 0.f, o = 0.f;
    for (int s = 0; s < 4; ++s) { const float wt = __builtin_amdgcn_exp2f(pp[s * 132] - M); L += wt * pp[s * 132 + 1]; o += wt * pp[s * 132 + 4 + d]; }
    os[b][d] = rbf(o / L);
  }
  __syncthreads();
  float a0 = 0.f, a1 = 0.f;
  for (int d = 0; d < DH; ++d) { const float wv = bf2f(wo[(size_t)(h * DH + d) * D + tid]); a0 += os[0][d] * wv; a1 += os[1][d] * wv; }
  yp[(h * 2 + 0) * D + tid] = a0; yp[(h * 2 + 1) * D + tid] = a1;
}

__device__ __forceinline__ float gelu_tanh(float x) {
  const float k = 0.7978845608028654f * (x + 0.044715f * x * x * x);
  return 0.5f * x * (1.f + tanhf(k));
}
__global__ void __launch_bounds__(256) k_mlp(const u16* x, const float* yp, const u16* wg, const u16* wu, const u16* wd, float* dp, float* hout) {
  __shared__ float hs[2][D], ln[2][D], red[4][4][64], act[2][64];
  const int tid = threadIdx.x, lane = tid & 63, w = tid >> 6;
  for (int i = tid; i < 2 * D; i += 256) {
    const int b = i / D, n = i % D; float s = 0.f;
    for (int h = 0; h < H; ++h) s += yp[(h * 2 + b) * D + n];
    const float hv = rbf(bf2f(x[i]) + rbf(s));
    hs[b][n] = hv;
    if (blockIdx.x == 0) hout[i] = hv;
  }
  __syncthreads();
  if (w < 2) ln_wave(hs[w], ln[w], lane);
  __syncthreads();
  const int f = blockIdx.x * 64 + lane;
  float g0 = 0.f, g1 = 0.f, u0 = 0.f, u1 = 0.f;
  for (int k = w; k < D; k += 4) {
    const float a = bf2f(wg[(size_t)k * DF + f]), b = bf2f(wu[(size_t)k * DF + f]);
    g0 += ln[0][k] * a; g1 += ln[1][k] * a; u0 += ln[0][k] * b; u1 += ln[1][k] * b;
  }
  red[w][0][lane] = g0; red[w][1][lane] = g1; red[w][2][lane] = u0; red[w][3][lane] = u1;
  __syncthreads();
  if (w < 2) {
    const float g = rbf(red[0][w][lane] + red[1][w][lane] + red[2][w][lane] + red[3][w][lane]);
    const float u = rbf(red[0][2 + w][lane] + red[1][2 + w][lane] + red[2][2 + w][lane] + red[3][2 + w][lane]);
    act[w][lane] = rbf(rbf(gelu_tanh(g)) * u);
  }
  __syncthreads();
  for (int n = tid; n < D; n += 256) {
    float a0 = 0.f, a1 = 0.f;
    for (int k = 0; k < 64; ++k) { const float wv = bf2f(wd[(size_t)(blockIdx.x * 64 + k) * D + n]); a0 += act[0][k] * wv; a1 += act[1][k] * wv; }
    dp[(blockIdx.x * 2 + 0) * D + n] = a0; dp[(blockIdx.x * 2 + 1) * D + n] = a1;
  }
}
__global__ void __launch_bounds__(64) k_fin(const float* dp, const float* h, u16* out) {
  const int i = blockIdx.x * 64 + threadIdx.x, b = i / D, n = i % D;
  float s = 0.f;
  for (int k = 0; k < 72; ++k) s += dp[(k * 2 + b) * D + n];
  out[i] = f2bf(h[i] + rbf(s));
}

extern "C" void kernel_launch(void* const* d_in, const int* in_sizes, int n_in, void* d_out, int out_size, void* d_ws, size_t ws_size, hipStream_t stream) {
  const u16* x = (const u16*)d_in[0]; const u16* K = (const u16*)d_in[1]; const u16* V = (const u16*)d_in[2];
  const u16* wq = (const u16*)d_in[3]; const u16* wo = (const u16*)d_in[4];
  const u16* wg = (const u16*)d_in[6]; const u16* wu = (const u16*)d_in[7]; const u16* wd = (const u16*)d_in[8];
  char* ws = (char*)d_ws;
  u16* q = (u16*)ws; float* part = (float*)(ws + 65536); float* yp = (float*)(ws + 65536 * 4); float* dp = (float*)(ws + 65536 * 8); float* h = (float*)(ws + 65536 * 16);
  k_q<<<64, 256, 0, stream>>>(x, wq, q);
  k_attn<<<256, 512, 0, stream>>>(q, K, V, part);
  k_wo<<<32, 576, 0, stream>>>(part, wo, yp);
  k_mlp<<<72, 256, 0, stream>>>(x, yp, wg, wu, wd, dp, h);
  k_fin<<<18, 64, 0, stream>>>(dp, h, (u16*)d_out);
}
```

```cpp
#include <hip/hip_runtime.h>
#include <hip/hip_bf16.h>
#include <stdint.h>

typedef unsigned short u16;
typedef unsigned v4u __attribute__((ext_vector_type(4)));
typedef unsigned v2u __attribute__((ext_vector_type(2)));
typedef float f32x4 __attribute__((ext_vector_type(4)));
typedef float f32x2 __attribute__((ext_vector_type(2)));
typedef short s16x4 __attribute__((ext_vector_type(4)));
typedef __bf16 bf16x2 __attribute__((ext_vector_type(2)));
typedef __bf16 bf16x8 __attribute__((ext_vector_type(8)));
#define LAS __attribute__((address_space(3)))
#define GAS __attribute__((address_space(1)))

constexpr int D = 576, H = 32, SK = 8192, DH = 128, HD = H * DH, DF = 4608;
constexpr int NWG = 256, NTHR = 576;
constexpr int T_WO = 122, T_MLP = 130;
#define NPRE 32
constexpr unsigned WS_E = 0, WS_S1 = 4096, S1_STR = 1152, WS_S2 = WS_S1 + 256 * S1_STR, WS_S3 = WS_S2 + 8 * D * 16, WS_END = WS_S3 + 192 * D * 16;
constexpr unsigned L_W = 0, L_WG = 0, L_WU = 27648, L_WD = 55296, L_WEND = 82944 + 256;
constexpr unsigned L_QP = L_WEND;
constexpr unsigned L_H = L_QP + 8 * 132 * 4;
constexpr unsigned L_RED = L_H + D * 8;
constexpr unsigned L_PRIV = L_RED + 8 * 80 * 8;
constexpr unsigned L_END = L_PRIV + 8 * 512;
constexpr int AUX_NT = 2, AUX_SC1 = 16, RS3 = 0x00020000;
constexpr unsigned SPIN_CAP = 1u << 17;

#define STAMP(i) do {} while (0)

__device__ __forceinline__ float bflo(unsigned u) { return __uint_as_float(u << 16); }
__device__ __forceinline__ float bfhi(unsigned u) { return __uint_as_float(u & 0xFFFF0000u); }
__device__ __forceinline__ unsigned cvtpk(float lo, float hi) {
  const bf16x2 r = {(__bf16)lo, (__bf16)hi};
  return __builtin_bit_cast(unsigned, r);
}
__device__ __forceinline__ float rbf(float f) { return bflo(cvtpk(f, f)); }
__device__ __forceinline__ float dot2(unsigned a, unsigned b, float c) {
  return __builtin_amdgcn_fdot2_f32_bf16(__builtin_bit_cast(bf16x2, a), __builtin_bit_cast(bf16x2, b), c, false);
}
template <int CTRL> __device__ __forceinline__ float dpp(float x) {
  return __builtin_bit_cast(float, __builtin_amdgcn_mov_dpp(__builtin_bit_cast(int, x), CTRL, 0xf, 0xf, true));
}
__device__ __forceinline__ float row_sum(float d) { d += dpp<0x128>(d); d += dpp<0x124>(d); d += dpp<0x122>(d); d += dpp<0x121>(d); return d; }
__device__ __forceinline__ float sw32_sum(float a, float b) {
  auto t = __builtin_amdgcn_permlane32_swap(__float_as_uint(a), __float_as_uint(b), false, false);
  return __uint_as_float(t[0]) + __uint_as_float(t[1]);
}
__device__ __forceinline__ float sw16_sum(float a, float b) {
  auto t = __builtin_amdgcn_permlane16_swap(__float_as_uint(a), __float_as_uint(b), false, false);
  return __uint_as_float(t[0]) + __uint_as_float(t[1]);
}
__device__ __forceinline__ float xrow_sum(float x) { x = sw16_sum(x, x); return sw32_sum(x, x); }
__device__ __forceinline__ float xrow_max(float x) {
  auto s = __builtin_amdgcn_permlane16_swap(__float_as_uint(x), __float_as_uint(x), false, false);
  x = fmaxf(__uint_as_float(s[0]), __uint_as_float(s[1]));
  auto t = __builtin_amdgcn_permlane32_swap(__float_as_uint(x), __float_as_uint(x), false, false);
  return fmaxf(__uint_as_float(t[0]), __uint_as_float(t[1]));
}
__device__ __forceinline__ float wave_sum(float x) { return xrow_sum(row_sum(x)); }
__device__ __forceinline__ f32x4 mfma(v4u a, v4u b, f32x4 c) {
  return __builtin_amdgcn_mfma_f32_16x16x32_bf16(__builtin_bit_cast(bf16x8, a), __builtin_bit_cast(bf16x8, b), c, 0, 0, 0);
}
__device__ __forceinline__ v4u trfrag(LAS unsigned char* L, unsigned a0, unsigned a1) {
  const s16x4 x = __builtin_amdgcn_ds_read_tr16_b64_v4i16((LAS s16x4*)(L + a0));
  const s16x4 y = __builtin_amdgcn_ds_read_tr16_b64_v4i16((LAS s16x4*)(L + a1));
  const v2u p = __builtin_bit_cast(v2u, x), q = __builtin_bit_cast(v2u, y);
  return v4u{p.x, p.y, q.x, q.y};
}
#define BAR() asm volatile("s_waitcnt lgkmcnt(0)\n\ts_barrier" ::: "memory")
#define LWAIT() asm volatile("s_waitcnt lgkmcnt(0)" ::: "memory")
#define SB() __builtin_amdgcn_sched_barrier(0)
#define BADC(v) (((v).y ^ T) | ((v).w ^ T))
constexpr float L2E = 1.4426950408889634f, SCALE = 0.08838834764831845f;

__device__ __forceinline__ void tile(const v4u (&kv)[8], const v4u qv, float& m, float& l, float (&acc)[8]) {
  float d[4];
#pragma unroll
  for (int i = 0; i < 4; ++i) {
    float t = dot2(kv[i].x, qv.x, 0.f); t = dot2(kv[i].y, qv.y, t); t = dot2(kv[i].z, qv.z, t); t = dot2(kv[i].w, qv.w, t);
    d[i] = row_sum(t);
  }
  unsigned p01 = cvtpk(d[0], d[1]), p23 = cvtpk(d[2], d[3]);
  p01 = cvtpk(bflo(p01) * SCALE, bfhi(p01) * SCALE); p23 = cvtpk(bflo(p23) * SCALE, bfhi(p23) * SCALE);
  const float s[4] = {bflo(p01) * L2E, bfhi(p01) * L2E, bflo(p23) * L2E, bfhi(p23) * L2E};
  const float mn = fmaxf(fmaxf(fmaxf(m, s[0]), s[1]), fmaxf(s[2], s[3]));
  const float al = __builtin_amdgcn_exp2f(m - mn);
  m = mn; l *= al;
#pragma unroll
  for (int j = 0; j < 8; ++j) acc[j] *= al;
#pragma unroll
  for (int i = 0; i < 4; ++i) {
    const float p = __builtin_amdgcn_exp2f(s[i] - mn);
    l += p;
#pragma unroll
    for (int j = 0; j < 4; ++j) {
      acc[2 * j] = fmaf(p, bflo(kv[4 + i][j]), acc[2 * j]);
      acc[2 * j + 1] = fmaf(p, bfhi(kv[4 + i][j]), acc[2 * j + 1]);
    }
  }
}
__device__ __forceinline__ void issue(v4u (&kv)[8], const u16* K, const u16* V, int tile_idx, bool live, int vo) {
  const int nrec = live ? (int)(128u << 20) : 0;
  const __amdgpu_buffer_rsrc_t rk = __builtin_amdgcn_make_buffer_rsrc((void*)K, 0, nrec, RS3);
  const __amdgpu_buffer_rsrc_t rv = __builtin_amdgcn_make_buffer_rsrc((void*)V, 0, nrec, RS3);
  const int so = live ? tile_idx * 4096 : 0;
#pragma unroll
  for (int i = 0; i < 4; ++i) kv[i] = __builtin_amdgcn_raw_buffer_load_b128(rk, vo + i * 1024, so, AUX_NT);
#pragma unroll
  for (int i = 0; i < 4; ++i) kv[4 + i] = __builtin_amdgcn_raw_buffer_load_b128(rv, vo + i * 1024, so, AUX_NT);
}
__device__ __forceinline__ float ln1(float v, float mb, float den) { return rbf(v - mb) / den; }
__device__ __forceinline__ float gelu_tanh(float x) {
  const float k = 0.7978845608028654f * (x + 0.044715f * x * x * x);
  const float e = __builtin_amdgcn_exp2f(k * (2.f * L2E));
  const float th = 1.f - 2.f / (e + 1.f);
  return 0.5f * x * (1.f + th);
}

__global__ void __launch_bounds__(NTHR, 3) fused(const u16* __restrict__ x, const u16* __restrict__ K, const u16* __restrict__ V, const u16* __restrict__ wq,
    const u16* __restrict__ wo, const u16* __restrict__ wg, const u16* __restrict__ wu, const u16* __restrict__ wd, u16* __restrict__ out, unsigned char* ws
    ) {
  extern __shared__ __attribute__((aligned(16))) unsigned char smem[];
  LAS unsigned char* L = (LAS unsigned char*)smem;
  const int tid = threadIdx.x, lane = tid & 63, w = __builtin_amdgcn_readfirstlane(tid >> 6), r = lane >> 4, c = lane & 15;
  const int g = blockIdx.x, x8 = g & 7, j = g >> 3, h = x8 * 4 + (j >> 3), b = (j >> 2) & 1, s = j & 3;
  const bool is_wo = (s == 0);
  const int cg = j >> 2, mi = x8 * 24 + (j >> 2) * 3 + (s - 1);
  const int NS = is_wo ? 16 : 18;
  STAMP(0);
  if (w == 8) {
    if (is_wo) {
      const unsigned char* src = (const unsigned char*)wo + ((size_t)(512 * x8 + lane / 9) * D + 72 * cg + 8 * (lane % 9)) * 2;
      int cc = lane % 9, p = 0;
      BAR();
      for (int k = 0; k < NS - 2; ++k) {
        for (int e = 0; e < 6 && p < 72; ++e, ++p) {
          __builtin_amdgcn_global_load_lds((const GAS void*)src, (LAS void*)(L + L_W + p * 1024), 16, 0, AUX_NT);
          src += 7 * 1152 + 16; if (++cc == 9) { cc = 0; src += 1152 - 144; }
        }
        asm volatile("s_barrier" ::: "memory");
      }
      asm volatile("s_waitcnt vmcnt(0)" ::: "memory");
      BAR(); BAR(); BAR(); BAR();
    } else {
      const unsigned goff = ((unsigned)(lane / 3) * DF + 24 * mi + 8 * (lane % 3)) * 2;
      const unsigned char* sg = (const unsigned char*)wg + goff;
      const unsigned char* su = (const unsigned char*)wu + goff;
      const unsigned char* sd = (const unsigned char*)wd + (size_t)24 * mi * D * 2 + lane * 16;
      int cc = lane % 3;
      BAR();
      for (int k = 0; k < NS - 2; ++k) asm volatile("s_barrier" ::: "memory");
      for (int p = 0; p < 27; ++p) {
        if (3 * p == (NPRE / 3) * 3) { BAR(); BAR(); }
        __builtin_amdgcn_global_load_lds((const GAS void*)sg, (LAS void*)(L + L_WG + p * 1024), 16, 0, AUX_NT);
        __builtin_amdgcn_global_load_lds((const GAS void*)su, (LAS void*)(L + L_WU + p * 1024), 16, 0, AUX_NT);
        __builtin_amdgcn_global_load_lds((const GAS void*)sd, (LAS void*)(L + L_WD + p * 1024), 16, 0, AUX_NT);
        int adv = 21 * 9216 + 16; if (++cc == 3) { cc = 0; adv += 9216 - 48; }
        sg += adv; su += adv; sd += 1024;
      }
      if (NPRE / 3 >= 27) { BAR(); BAR(); }
      asm volatile("s_waitcnt vmcnt(0)" ::: "memory");
      STAMP(5);
      BAR(); BAR(); BAR();
    }
    return;
  }
  const __amdgpu_buffer_rsrc_t rws = __builtin_amdgcn_make_buffer_rsrc((void*)ws, 0, (int)WS_END, RS3);
  const unsigned E = __builtin_amdgcn_raw_buffer_load_b32(rws, 0, WS_E, AUX_SC1);
  const int lo = (b * H + h) * 512 + (is_wo ? 0 : T_WO + (s - 1) * T_MLP), ntl = is_wo ? T_WO : T_MLP;
  const int ne = NS - ((ntl - w + 7) >> 3);
  const int vo = lane * 16;
  v4u A[8], Bf[8];
  v4u qv;
  {
    const __amdgpu_buffer_rsrc_t rx = __builtin_amdgcn_make_buffer_rsrc((void*)(x + b * D), 0, D * 2, RS3);
    const v4u xa = __builtin_amdgcn_raw_buffer_load_b128(rx, vo, 0, 0), xb = __builtin_amdgcn_raw_buffer_load_b128(rx, vo + 1024, 0, 0);
    const int xo = (72 * w + 18 * r) * 2;
    const v4u xr0 = __builtin_amdgcn_raw_buffer_load_b128(rx, xo, 0, 0), xr1 = __builtin_amdgcn_raw_buffer_load_b128(rx, xo + 16, 0, 0);
    const unsigned xr2 = __builtin_amdgcn_raw_buffer_load_b32(rx, xo + 32, 0, 0);
    const __amdgpu_buffer_rsrc_t rq = __builtin_amdgcn_make_buffer_rsrc((void*)wq, 0, D * HD * 2, RS3);
    const __amdgpu_buffer_rsrc_t rk = __builtin_amdgcn_make_buffer_rsrc((void*)K, 0, (int)(128u << 20), RS3);
    const __amdgpu_buffer_rsrc_t rv = __builtin_amdgcn_make_buffer_rsrc((void*)V, 0, (int)(128u << 20), RS3);
    const int qo = (18 * r * HD + 8 * c) * 2, qs = (72 * w * HD + h * DH) * 2, so0 = (lo + w) * 4096;
    v4u wv[18];
    __builtin_amdgcn_s_setprio(3);
#pragma unroll
    for (int i = 0; i < 18; ++i) {
      wv[i] = __builtin_amdgcn_raw_buffer_load_b128(rq, qo, qs + i * HD * 2, AUX_NT);
      if (i % 2 == 1 && i / 2 < 8) { const int t = i / 2; A[t] = __builtin_amdgcn_raw_buffer_load_b128(t < 4 ? rk : rv, vo + (t & 3) * 1024, so0, AUX_NT); }
      SB();
    }
    __builtin_amdgcn_s_setprio(0);
    float sum = 0.f;
#pragma unroll
    for (int i = 0; i < 4; ++i) sum += (bflo(xa[i]) + bfhi(xa[i])) + (bflo(xb[i]) + bfhi(xb[i]));
    const float mean = wave_sum(sum) * (1.f / D);
    float sq = 0.f, sq2 = 0.f;
#pragma unroll
    for (int i = 0; i < 4; ++i) {
      const float d0 = bflo(xa[i]) - mean, d1 = bfhi(xa[i]) - mean, d2 = bflo(xb[i]) - mean, d3 = bfhi(xb[i]) - mean;
      sq += d0 * d0 + d1 * d1; sq2 += d2 * d2 + d3 * d3;
    }
    sq += lane < 8 ? sq2 : 0.f;
    const float var = wave_sum(sq) * (1.f / D);
    const float mb = rbf(mean), den = rbf(sqrtf(rbf(rbf(var) + 1e-5f)));
    const unsigned xr[9] = {xr0.x, xr0.y, xr0.z, xr0.w, xr1.x, xr1.y, xr1.z, xr1.w, xr2};
    float qa[8];
#pragma unroll
    for (int i = 0; i < 8; ++i) qa[i] = 0.f;
#pragma unroll
    for (int p = 0; p < 9; ++p) {
      if (p == 4) { SB(); issue(Bf, K, V, lo + w + 8, true, vo); SB(); }
      const unsigned lp = cvtpk(ln1(bflo(xr[p]), mb, den), ln1(bfhi(xr[p]), mb, den));
#pragma unroll
      for (int d = 0; d < 4; ++d) {
        qa[2 * d] = dot2(__builtin_amdgcn_perm(wv[2 * p + 1][d], wv[2 * p][d], 0x05040100u), lp, qa[2 * d]);
        qa[2 * d + 1] = dot2(__builtin_amdgcn_perm(wv[2 * p + 1][d], wv[2 * p][d], 0x07060302u), lp, qa[2 * d + 1]);
      }
    }
    const float z0 = sw32_sum(qa[0], qa[4]), z1 = sw32_sum(qa[1], qa[5]), z2 = sw32_sum(qa[2], qa[6]), z3 = sw32_sum(qa[3], qa[7]);
    *(LAS f32x2*)(L + L_QP + (w * 128 + 8 * c + 2 * r) * 4) = f32x2{sw16_sum(z0, z2), sw16_sum(z1, z3)};
    STAMP(1);
    BAR();
    f32x4 q0 = {0.f, 0.f, 0.f, 0.f}, q1 = q0;
#pragma unroll
    for (int i = 0; i < 8; ++i) { q0 += *(LAS f32x4*)(L + L_QP + (i * 128 + 8 * c) * 4); q1 += *(LAS f32x4*)(L + L_QP + (i * 128 + 8 * c + 4) * 4); }
    qv = v4u{cvtpk(q0.x, q0.y), cvtpk(q0.z, q0.w), cvtpk(q1.x, q1.y), cvtpk(q1.z, q1.w)};
    STAMP(2);
  }
  float m = -INFINITY, l = 0.f, acc[8];
#pragma unroll
  for (int i = 0; i < 8; ++i) acc[i] = 0.f;
  for (int k = 0; k < NS - 2; k += 2) {
    if (k < 2 || k >= 2 + ne) tile(A, qv, m, l, acc);
    SB();
    issue(A, K, V, lo + w + 8 * (k + 2 - ne), k + 2 >= 2 + ne, vo);
    SB();
    asm volatile("s_barrier" ::: "memory");
    if (k + 1 < 2 || k + 1 >= 2 + ne) tile(Bf, qv, m, l, acc);
    SB();
    issue(Bf, K, V, lo + w + 8 * (k + 3 - ne), k + 3 >= 2 + ne, vo);
    SB();
    asm volatile("s_barrier" ::: "memory");
  }
  tile(A, qv, m, l, acc);
  tile(Bf, qv, m, l, acc);
  STAMP(3);
  const unsigned T = __builtin_amdgcn_readfirstlane(E) + 1u;
  {
    const float M = xrow_max(m), f = __builtin_amdgcn_exp2f(m - M);
    const float Lw = xrow_sum(l * f);
#pragma unroll
    for (int i = 0; i < 8; ++i) acc[i] *= f;
    const float z0 = sw32_sum(acc[0], acc[4]), z1 = sw32_sum(acc[1], acc[5]), z2 = sw32_sum(acc[2], acc[6]), z3 = sw32_sum(acc[3], acc[7]);
    *(LAS f32x2*)(L + L_QP + (w * 132 + 4 + 8 * c + 2 * r) * 4) = f32x2{sw16_sum(z0, z2), sw16_sum(z1, z3)};
    if (lane == 0) *(LAS f32x2*)(L + L_QP + (w * 132) * 4) = f32x2{M, Lw};
    BAR();
    if (w == 0) {
      f32x2 ml[8], o[8];
#pragma unroll
      for (int i = 0; i < 8; ++i) { ml[i] = *(LAS f32x2*)(L + L_QP + (i * 132) * 4); o[i] = *(LAS f32x2*)(L + L_QP + (i * 132 + 4 + 2 * lane) * 4); }
      float MM = ml[0].x;
#pragma unroll
      for (int i = 1; i < 8; ++i) MM = fmaxf(MM, ml[i].x);
      float LL = 0.f, o0 = 0.f, o1 = 0.f;
#pragma unroll
      for (int i = 0; i < 8; ++i) { const float wt = __builtin_amdgcn_exp2f(ml[i].x - MM); LL = fmaf(wt, ml[i].y, LL); o0 = fmaf(wt, o[i].x, o0); o1 = fmaf(wt, o[i].y, o1); }
      const int so = WS_S1 + ((h * 2 + b) * 4 + s) * S1_STR;
      __builtin_amdgcn_raw_buffer_store_b128(v4u{__float_as_uint(o0), T, __float_as_uint(o1), T}, rws, vo, so, AUX_SC1);
      if (lane == 0) __builtin_amdgcn_raw_buffer_store_b128(v4u{__float_as_uint(MM), T, __float_as_uint(LL), T}, rws, 1024, so, AUX_SC1);
      STAMP(4);
    }
    BAR();
  }
  const int q4 = c >> 2, p4 = c & 3;
  LAS unsigned char* priv = L + L_PRIV + w * 512;
  if (is_wo) {
    BAR();
    v4u bf[2][5];
#pragma unroll
    for (int ks = 0; ks < 2; ++ks)
#pragma unroll
      for (int t = 0; t < 5; ++t) { const unsigned a = L_W + (64 * w + 32 * ks + 8 * r + q4) * 144 + (16 * t + 4 * p4) * 2; bf[ks][t] = trfrag(L, a, a + 4 * 144); }
    const int pb = lane >> 5, base = ((4 * x8 + (w >> 1)) * 2 + pb) * 4 * S1_STR, cj = (32 * (w & 1) + (lane & 31)) * 16;
    v4u ov[4], mv[4];
#pragma nounroll
    for (unsigned spin = 0; spin < SPIN_CAP; ++spin) {
      asm volatile("" ::: "memory");
#pragma unroll
      for (int i = 0; i < 4; ++i) {
        ov[i] = __builtin_amdgcn_raw_buffer_load_b128(rws, base + i * S1_STR + cj, WS_S1, AUX_SC1);
        mv[i] = __builtin_amdgcn_raw_buffer_load_b128(rws, base + i * S1_STR + 1024, WS_S1, AUX_SC1);
      }
      unsigned bad = 0u;
#pragma unroll
      for (int i = 0; i < 4; ++i) bad |= BADC(ov[i]) | BADC(mv[i]);
      if (__all(bad == 0u)) break;
      __builtin_amdgcn_s_sleep(1);
    }
    STAMP(6);
    const float M = fmaxf(fmaxf(__uint_as_float(mv[0].x), __uint_as_float(mv[1].x)), fmaxf(__uint_as_float(mv[2].x), __uint_as_float(mv[3].x)));
    float LL = 0.f, o0 = 0.f, o1 = 0.f;
#pragma unroll
    for (int i = 0; i < 4; ++i) {
      const float wt = __builtin_amdgcn_exp2f(__uint_as_float(mv[i].x) - M);
      LL = fmaf(wt, __uint_as_float(mv[i].z), LL); o0 = fmaf(wt, __uint_as_float(ov[i].x), o0); o1 = fmaf(wt, __uint_as_float(ov[i].z), o1);
    }
    *(LAS unsigned*)(priv + lane * 4) = cvtpk(o0 / LL, o1 / LL);
    LWAIT();
    const v4u a0 = *(LAS v4u*)(priv + ((c & 1) * 32 + 4 * r) * 4), a1 = *(LAS v4u*)(priv + ((c & 1) * 32 + 16 + 4 * r) * 4);
#pragma unroll
    for (int t = 0; t < 5; ++t) {
      f32x4 d = mfma(a0, bf[0][t], f32x4{0.f, 0.f, 0.f, 0.f});
      d = mfma(a1, bf[1][t], d);
      if (lane < 16) *(LAS f32x2*)(L + L_RED + (w * 80 + 16 * t + lane) * 8) = f32x2{d.x, d.y};
    }
    BAR();
    if (tid < 72) {
      f32x2 y = {0.f, 0.f};
#pragma unroll
      for (int i = 0; i < 8; ++i) y += *(LAS f32x2*)(L + L_RED + (i * 80 + tid) * 8);
      __builtin_amdgcn_raw_buffer_store_b128(v4u{__float_as_uint(y.x), T, __float_as_uint(y.y), T}, rws, tid * 16, WS_S2 + (x8 * D + 72 * cg) * 16, AUX_SC1);
    }
    STAMP(7);
    return;
  }
  const int n1 = 72 * w + lane, n2 = 72 * w + 64 + (lane & 7);
  const float xa0 = bflo(x[n1]), xa1 = bflo(x[D + n1]), xb0 = bflo(x[n2]), xb1 = bflo(x[D + n2]);
  BAR();
  v4u gf[3][3], df[5];
#pragma unroll
  for (int i = 0; i < 3; ++i) {
    const int ks = (i < 2 || w < 2) ? w + 8 * i : 0;
#pragma unroll
    for (int t = 0; t < 3; ++t) {
      const int col = 16 * t + 4 * p4;
      const unsigned a = (col < 24 ? L_WG + col * 2 : L_WU + (col - 24) * 2) + (32 * ks + 8 * r + q4) * 48;
      gf[i][t] = trfrag(L, a, a + 4 * 48);
    }
  }
#pragma unroll
  for (int i = 0; i < 5; ++i) {
    const int t = (i < 4 || w < 4) ? w + 8 * i : 0;
    const unsigned a = L_WD + ((r < 3 ? 8 * r : 0) + q4) * 1152 + (16 * t + 4 * p4) * 2;
    const v4u f = trfrag(L, a, a + 4 * 1152);
    df[i] = r < 3 ? f : v4u{0u, 0u, 0u, 0u};
  }
  {
    v4u yv[8], ye;
#pragma nounroll
    for (unsigned spin = 0; spin < SPIN_CAP; ++spin) {
      asm volatile("" ::: "memory");
#pragma unroll
      for (int i = 0; i < 8; ++i) yv[i] = __builtin_amdgcn_raw_buffer_load_b128(rws, n1 * 16, WS_S2 + i * D * 16, AUX_SC1);
      ye = __builtin_amdgcn_raw_buffer_load_b128(rws, ((lane >> 3) * D + n2) * 16, WS_S2, AUX_SC1);
      unsigned bad = BADC(ye);
#pragma unroll
      for (int i = 0; i < 8; ++i) bad |= BADC(yv[i]);
      if (__all(bad == 0u)) break;
      __builtin_amdgcn_s_sleep(1);
    }
    STAMP(6);
    float y0 = 0.f, y1 = 0.f;
#pragma unroll
    for (int i = 0; i < 8; ++i) { y0 += __uint_as_float(yv[i].x); y1 += __uint_as_float(yv[i].z); }
    float e0 = __uint_as_float(ye.x), e1 = __uint_as_float(ye.z);
    e0 += dpp<0x128>(e0); e1 += dpp<0x128>(e1);
    e0 = xrow_sum(e0); e1 = xrow_sum(e1);
    *(LAS f32x2*)(L + L_H + n1 * 8) = f32x2{rbf(xa0 + rbf(y0)), rbf(xa1 + rbf(y1))};
    if (lane < 8) *(LAS f32x2*)(L + L_H + n2 * 8) = f32x2{rbf(xb0 + rbf(e0)), rbf(xb1 + rbf(e1))};
  }
  BAR();
  {
    f32x2 hv[9];
#pragma unroll
    for (int i = 0; i < 9; ++i) hv[i] = *(LAS f32x2*)(L + L_H + (64 * i + lane) * 8);
    float mine[3];
#pragma unroll
    for (int i = 0; i < 3; ++i) { const int ks = (i < 2 || w < 2) ? w + 8 * i : 0; mine[i] = *(LAS float*)(L + L_H + (32 * ks + (lane & 31)) * 8 + (lane >> 5) * 4); }
    f32x2 sm = hv[0];
#pragma unroll
    for (int i = 1; i < 9; ++i) sm += hv[i];
    const float m0 = wave_sum(sm.x) * (1.f / D), m1 = wave_sum(sm.y) * (1.f / D);
    float s0 = 0.f, s1 = 0.f;
#pragma unroll
    for (int i = 0; i < 9; ++i) { const float d0 = hv[i].x - m0, d1 = hv[i].y - m1; s0 = fmaf(d0, d0, s0); s1 = fmaf(d1, d1, s1); }
    const float v0 = wave_sum(s0) * (1.f / D), v1 = wave_sum(s1) * (1.f / D);
    const float mb = rbf(lane < 32 ? m0 : m1), den = rbf(sqrtf(rbf(rbf(lane < 32 ? v0 : v1) + 1e-5f)));
#pragma unroll
    for (int i = 0; i < 3; ++i) *(LAS u16*)(priv + i * 128 + lane * 2) = (u16)cvtpk(ln1(mine[i], mb, den), 0.f);
    LWAIT();
    f32x4 d[3];
#pragma unroll
    for (int t = 0; t < 3; ++t) d[t] = f32x4{0.f, 0.f, 0.f, 0.f};
#pragma unroll
    for (int i = 0; i < 3; ++i) {
      v4u a = *(LAS v4u*)(priv + i * 128 + (c & 1) * 64 + r * 16);
      if (i == 2 && w >= 2) a = v4u{0u, 0u, 0u, 0u};
#pragma unroll
      for (int t = 0; t < 3; ++t) d[t] = mfma(a, gf[i][t], d[t]);
    }
    if (lane < 16) {
#pragma unroll
      for (int t = 0; t < 3; ++t) *(LAS f32x2*)(L + L_RED + (w * 48 + 16 * t + lane) * 8) = f32x2{d[t].x, d[t].y};
    }
  }
  BAR();
  {
    const int pb = lane >> 5, cc = (lane & 31) < 24 ? (lane & 31) : 0;
    float gg = 0.f, uu = 0.f;
#pragma unroll
    for (int i = 0; i < 8; ++i) { gg += *(LAS float*)(L + L_RED + (i * 48 + cc) * 8 + pb * 4); uu += *(LAS float*)(L + L_RED + (i * 48 + 24 + cc) * 8 + pb * 4); }
    const float act = (lane & 31) < 24 ? rbf(gelu_tanh(rbf(gg))) * rbf(uu) : 0.f;
    *(LAS u16*)(priv + 384 + lane * 2) = (u16)cvtpk(act, 0.f);
    LWAIT();
    const v4u a = *(LAS v4u*)(priv + 384 + (c & 1) * 64 + r * 16);
#pragma unroll
    for (int i = 0; i < 5; ++i) {
      const f32x4 d = mfma(a, df[i], f32x4{0.f, 0.f, 0.f, 0.f});
      if (lane < 16 && (i < 4 || w < 4))
        __builtin_amdgcn_raw_buffer_store_b128(v4u{__float_as_uint(d.x), T, __float_as_uint(d.y), T}, rws, lane * 16, WS_S3 + (mi * D + 16 * (w + 8 * i)) * 16, AUX_SC1);
    }
  }
  STAMP(7);
  if (w == 7 && mi < 144) {
    const int n = 4 * mi + (lane & 3);
    const f32x2 hres = *(LAS f32x2*)(L + L_H + n * 8);
    v4u zv[12];
#pragma nounroll
    for (unsigned spin = 0; spin < SPIN_CAP; ++spin) {
      asm volatile("" ::: "memory");
#pragma unroll
      for (int i = 0; i < 12; ++i) zv[i] = __builtin_amdgcn_raw_buffer_load_b128(rws, ((lane >> 2) * D + n) * 16, WS_S3 + i * 16 * D * 16, AUX_SC1);
      unsigned bad = 0u;
#pragma unroll
      for (int i = 0; i < 12; ++i) bad |= BADC(zv[i]);
      if (__all(bad == 0u)) break;
    }
    STAMP(8);
    float z0 = 0.f, z1 = 0.f;
#pragma unroll
    for (int i = 0; i < 12; ++i) { z0 += __uint_as_float(zv[i].x); z1 += __uint_as_float(zv[i].z); }
    z0 += dpp<0x124>(z0); z1 += dpp<0x124>(z1); z0 += dpp<0x128>(z0); z1 += dpp<0x128>(z1);
    z0 = xrow_sum(z0); z1 = xrow_sum(z1);
    if (lane < 8) out[(lane >> 2) * D + n] = (u16)cvtpk(lane < 4 ? hres.x + rbf(z0) : hres.y + rbf(z1), 0.f);
    if (mi == 0 && lane == 0) __builtin_amdgcn_raw_buffer_store_b32(T, rws, 0, WS_E, AUX_SC1);
    STAMP(9);
  }
}

extern "C" void kernel_launch(void* const* d_in, const int* in_sizes, int n_in, void* d_out, int out_size, void* d_ws, size_t ws_size, hipStream_t stream) {
  (void)hipFuncSetAttribute((const void*)fused, hipFuncAttributeMaxDynamicSharedMemorySize, L_END);
  fused<<<NWG, NTHR, L_END, stream>>>((const u16*)d_in[0], (const u16*)d_in[1], (const u16*)d_in[2], (const u16*)d_in[3], (const u16*)d_in[4],
      (const u16*)d_in[6], (const u16*)d_in[7], (const u16*)d_in[8], (u16*)d_out, (unsigned char*)d_ws
      );
}
```

```cpp
#define STAMPS
#include <cstdio>
#include <vector>
#include <algorithm>
#include <hip/hip_runtime.h>
#include <hip/hip_bf16.h>
#include <stdint.h>

typedef unsigned short u16;
typedef unsigned v4u __attribute__((ext_vector_type(4)));
typedef unsigned v2u __attribute__((ext_vector_type(2)));
typedef float f32x4 __attribute__((ext_vector_type(4)));
typedef float f32x2 __attribute__((ext_vector_type(2)));
typedef short s16x4 __attribute__((ext_vector_type(4)));
typedef __bf16 bf16x2 __attribute__((ext_vector_type(2)));
typedef __bf16 bf16x8 __attribute__((ext_vector_type(8)));
#define LAS __attribute__((address_space(3)))
#define GAS __attribute__((address_space(1)))

constexpr int D = 576, H = 32, SK = 8192, DH = 128, HD = H * DH, DF = 4608;
constexpr int NWG = 256, NTHR = 576;
constexpr int T_WO = 122, T_MLP = 130;
#define NPRE 32
constexpr unsigned WS_E = 0, WS_S1 = 4096, S1_STR = 1152, WS_S2 = WS_S1 + 256 * S1_STR, WS_S3 = WS_S2 + 8 * D * 16, WS_END = WS_S3 + 192 * D * 16;
constexpr unsigned L_W = 0, L_WG = 0, L_WU = 27648, L_WD = 55296, L_WEND = 82944 + 256;
constexpr unsigned L_QP = L_WEND;
constexpr unsigned L_H = L_QP + 8 * 132 * 4;
constexpr unsigned L_RED = L_H + D * 8;
constexpr unsigned L_PRIV = L_RED + 8 * 80 * 8;
constexpr unsigned L_END = L_PRIV + 8 * 512;
constexpr int AUX_NT = 2, AUX_SC1 = 16, RS3 = 0x00020000;
constexpr unsigned SPIN_CAP = 1u << 17;

#define STAMP(i) do { if ((threadIdx.x & 63) == 0) st[(blockIdx.x * 9 + (threadIdx.x >> 6)) * 16 + (i)] = __builtin_amdgcn_s_memrealtime(); } while (0)

__device__ __forceinline__ float bflo(unsigned u) { return __uint_as_float(u << 16); }
__device__ __forceinline__ float bfhi(unsigned u) { return __uint_as_float(u & 0xFFFF0000u); }
__device__ __forceinline__ unsigned cvtpk(float lo, float hi) {
  const bf16x2 r = {(__bf16)lo, (__bf16)hi};
  return __builtin_bit_cast(unsigned, r);
}
__device__ __forceinline__ float rbf(float f) { return bflo(cvtpk(f, f)); }
__device__ __forceinline__ float dot2(unsigned a, unsigned b, float c) {
  return __builtin_amdgcn_fdot2_f32_bf16(__builtin_bit_cast(bf16x2, a), __builtin_bit_cast(bf16x2, b), c, false);
}
template <int CTRL> __device__ __forceinline__ float dpp(float x) {
  return __builtin_bit_cast(float, __builtin_amdgcn_mov_dpp(__builtin_bit_cast(int, x), CTRL, 0xf, 0xf, true));
}
__device__ __forceinline__ float row_sum(float d) { d += dpp<0x128>(d); d += dpp<0x124>(d); d += dpp<0x122>(d); d += dpp<0x121>(d); return d; }
__device__ __forceinline__ float sw32_sum(float a, float b) {
  auto t = __builtin_amdgcn_permlane32_swap(__float_as_uint(a), __float_as_uint(b), false, false);
  return __uint_as_float(t[0]) + __uint_as_float(t[1]);
}
__device__ __forceinline__ float sw16_sum(float a, float b) {
  auto t = __builtin_amdgcn_permlane16_swap(__float_as_uint(a), __float_as_uint(b), false, false);
  return __uint_as_float(t[0]) + __uint_as_float(t[1]);
}
__device__ __forceinline__ float xrow_sum(float x) { x = sw16_sum(x, x); return sw32_sum(x, x); }
__device__ __forceinline__ float xrow_max(float x) {
  auto s = __builtin_amdgcn_permlane16_swap(__float_as_uint(x), __float_as_uint(x), false, false);
  x = fmaxf(__uint_as_float(s[0]), __uint_as_float(s[1]));
  auto t = __builtin_amdgcn_permlane32_swap(__float_as_uint(x), __float_as_uint(x), false, false);
  return fmaxf(__uint_as_float(t[0]), __uint_as_float(t[1]));
}
__device__ __forceinline__ float wave_sum(float x) { return xrow_sum(row_sum(x)); }
__device__ __forceinline__ f32x4 mfma(v4u a, v4u b, f32x4 c) {
  return __builtin_amdgcn_mfma_f32_16x16x32_bf16(__builtin_bit_cast(bf16x8, a), __builtin_bit_cast(bf16x8, b), c, 0, 0, 0);
}
__device__ __forceinline__ v4u trfrag(LAS unsigned char* L, unsigned a0, unsigned a1) {
  const s16x4 x = __builtin_amdgcn_ds_read_tr16_b64_v4i16((LAS s16x4*)(L + a0));
  const s16x4 y = __builtin_amdgcn_ds_read_tr16_b64_v4i16((LAS s16x4*)(L + a1));
  const v2u p = __builtin_bit_cast(v2u, x), q = __builtin_bit_cast(v2u, y);
  return v4u{p.x, p.y, q.x, q.y};
}
#define BAR() asm volatile("s_waitcnt lgkmcnt(0)\n\ts_barrier" ::: "memory")
#define LWAIT() asm volatile("s_waitcnt lgkmcnt(0)" ::: "memory")
#define SB() __builtin_amdgcn_sched_barrier(0)
#define BADC(v) (((v).y ^ T) | ((v).w ^ T))
constexpr float L2E = 1.4426950408889634f, SCALE = 0.08838834764831845f;

__device__ __forceinline__ void tile(const v4u (&kv)[8], const v4u qv, float& m, float& l, float (&acc)[8]) {
  float d[4];
#pragma unroll
  for (int i = 0; i < 4; ++i) {
    float t = dot2(kv[i].x, qv.x, 0.f); t = dot2(kv[i].y, qv.y, t); t = dot2(kv[i].z, qv.z, t); t = dot2(kv[i].w, qv.w, t);
    d[i] = row_sum(t);
  }
  unsigned p01 = cvtpk(d[0], d[1]), p23 = cvtpk(d[2], d[3]);
  p01 = cvtpk(bflo(p01) * SCALE, bfhi(p01) * SCALE); p23 = cvtpk(bflo(p23) * SCALE, bfhi(p23) * SCALE);
  const float s[4] = {bflo(p01) * L2E, bfhi(p01) * L2E, bflo(p23) * L2E, bfhi(p23) * L2E};
  const float mn = fmaxf(fmaxf(fmaxf(m, s[0]), s[1]), fmaxf(s[2], s[3]));
  const float al = __builtin_amdgcn_exp2f(m - mn);
  m = mn; l *= al;
#pragma unroll
  for (int j = 0; j < 8; ++j) acc[j] *= al;
#pragma unroll
  for (int i = 0; i < 4; ++i) {
    const float p = __builtin_amdgcn_exp2f(s[i] - mn);
    l += p;
#pragma unroll
    for (int j = 0; j < 4; ++j) {
      acc[2 * j] = fmaf(p, bflo(kv[4 + i][j]), acc[2 * j]);
      acc[2 * j + 1] = fmaf(p, bfhi(kv[4 + i][j]), acc[2 * j + 1]);
    }
  }
}
__device__ __forceinline__ void issue(v4u (&kv)[8], const u16* K, const u16* V, int tile_idx, bool live, int vo) {
  const int nrec = live ? (int)(128u << 20) : 0;
  const __amdgpu_buffer_rsrc_t rk = __builtin_amdgcn_make_buffer_rsrc((void*)K, 0, nrec, RS3);
  const __amdgpu_buffer_rsrc_t rv = __builtin_amdgcn_make_buffer_rsrc((void*)V, 0, nrec, RS3);
  const int so = live ? tile_idx * 4096 : 0;
#pragma unroll
  for (int i = 0; i < 4; ++i) kv[i] = __builtin_amdgcn_raw_buffer_load_b128(rk, vo + i * 1024, so, AUX_NT);
#pragma unroll
  for (int i = 0; i < 4; ++i) kv[4 + i] = __builtin_amdgcn_raw_buffer_load_b128(rv, vo + i * 1024, so, AUX_NT);
}
__device__ __forceinline__ float ln1(float v, float mb, float den) { return rbf(v - mb) / den; }
__device__ __forceinline__ float gelu_tanh(float x) {
  const float k = 0.7978845608028654f * (x + 0.044715f * x * x * x);
  const float e = __builtin_amdgcn_exp2f(k * (2.f * L2E));
  const float th = 1.f - 2.f / (e + 1.f);
  return 0.5f * x * (1.f + th);
}

__global__ void __launch_bounds__(NTHR, 3) fused(const u16* __restrict__ x, const u16* __restrict__ K, const u16* __restrict__ V, const u16* __restrict__ wq,
    const u16* __restrict__ wo, const u16* __restrict__ wg, const u16* __restrict__ wu, const u16* __restrict__ wd, u16* __restrict__ out, unsigned char* ws
    , unsigned long long* st
    ) {
  extern __shared__ __attribute__((aligned(16))) unsigned char smem[];
  LAS unsigned char* L = (LAS unsigned char*)smem;
  const int tid = threadIdx.x, lane = tid & 63, w = __builtin_amdgcn_readfirstlane(tid >> 6), r = lane >> 4, c = lane & 15;
  const int g = blockIdx.x, x8 = g & 7, j = g >> 3, h = x8 * 4 + (j >> 3), b = (j >> 2) & 1, s = j & 3;
  const bool is_wo = (s == 0);
  const int cg = j >> 2, mi = x8 * 24 + (j >> 2) * 3 + (s - 1);
  const int NS = is_wo ? 16 : 18;
  STAMP(0);
  if (w == 8) {
    if (is_wo) {
      const unsigned char* src = (const unsigned char*)wo + ((size_t)(512 * x8 + lane / 9) * D + 72 * cg + 8 * (lane % 9)) * 2;
      int cc = lane % 9, p = 0;
      BAR();
      for (int k = 0; k < NS - 2; ++k) {
        for (int e = 0; e < 6 && p < 72; ++e, ++p) {
          __builtin_amdgcn_global_load_lds((const GAS void*)src, (LAS void*)(L + L_W + p * 1024), 16, 0, AUX_NT);
          src += 7 * 1152 + 16; if (++cc == 9) { cc = 0; src += 1152 - 144; }
        }
        asm volatile("s_barrier" ::: "memory");
      }
      asm volatile("s_waitcnt vmcnt(0)" ::: "memory");
      BAR(); BAR(); BAR(); BAR();
    } else {
      const unsigned goff = ((unsigned)(lane / 3) * DF + 24 * mi + 8 * (lane % 3)) * 2;
      const unsigned char* sg = (const unsigned char*)wg + goff;
      const unsigned char* su = (const unsigned char*)wu + goff;
      const unsigned char* sd = (const unsigned char*)wd + (size_t)24 * mi * D * 2 + lane * 16;
      int cc = lane % 3;
      BAR();
      for (int k = 0; k < NS - 2; ++k) asm volatile("s_barrier" ::: "memory");
      for (int p = 0; p < 27; ++p) {
        if (3 * p == (NPRE / 3) * 3) { BAR(); BAR(); }
        __builtin_amdgcn_global_load_lds((const GAS void*)sg, (LAS void*)(L + L_WG + p * 1024), 16, 0, AUX_NT);
        __builtin_amdgcn_global_load_lds((const GAS void*)su, (LAS void*)(L + L_WU + p * 1024), 16, 0, AUX_NT);
        __builtin_amdgcn_global_load_lds((const GAS void*)sd, (LAS void*)(L + L_WD + p * 1024), 16, 0, AUX_NT);
        int adv = 21 * 9216 + 16; if (++cc == 3) { cc = 0; adv += 9216 - 48; }
        sg += adv; su += adv; sd += 1024;
      }
      if (NPRE / 3 >= 27) { BAR(); BAR(); }
      asm volatile("s_waitcnt vmcnt(0)" ::: "memory");
      STAMP(5);
      BAR(); BAR(); BAR();
    }
    return;
  }
  const __amdgpu_buffer_rsrc_t rws = __builtin_amdgcn_make_buffer_rsrc((void*)ws, 0, (int)WS_END, RS3);
  const unsigned E = __builtin_amdgcn_raw_buffer_load_b32(rws, 0, WS_E, AUX_SC1);
  const int lo = (b * H + h) * 512 + (is_wo ? 0 : T_WO + (s - 1) * T_MLP), ntl = is_wo ? T_WO : T_MLP;
  const int ne = NS - ((ntl - w + 7) >> 3);
  const int vo = lane * 16;
  v4u A[8], Bf[8];
  v4u qv;
  {
    const __amdgpu_buffer_rsrc_t rx = __builtin_amdgcn_make_buffer_rsrc((void*)(x + b * D), 0, D * 2, RS3);
    const v4u xa = __builtin_amdgcn_raw_buffer_load_b128(rx, vo, 0, 0), xb = __builtin_amdgcn_raw_buffer_load_b128(rx, vo + 1024, 0, 0);
    const int xo = (72 * w + 18 * r) * 2;
    const v4u xr0 = __builtin_amdgcn_raw_buffer_load_b128(rx, xo, 0, 0), xr1 = __builtin_amdgcn_raw_buffer_load_b128(rx, xo + 16, 0, 0);
    const unsigned xr2 = __builtin_amdgcn_raw_buffer_load_b32(rx, xo + 32, 0, 0);
    const __amdgpu_buffer_rsrc_t rq = __builtin_amdgcn_make_buffer_rsrc((void*)wq, 0, D * HD * 2, RS3);
    const __amdgpu_buffer_rsrc_t rk = __builtin_amdgcn_make_buffer_rsrc((void*)K, 0, (int)(128u << 20), RS3);
    const __amdgpu_buffer_rsrc_t rv = __builtin_amdgcn_make_buffer_rsrc((void*)V, 0, (int)(128u << 20), RS3);
    const int qo = (18 * r * HD + 8 * c) * 2, qs = (72 * w * HD + h * DH) * 2, so0 = (lo + w) * 4096;
    v4u wv[18];
    __builtin_amdgcn_s_setprio(3);
#pragma unroll
    for (int i = 0; i < 18; ++i) {
      wv[i] = __builtin_amdgcn_raw_buffer_load_b128(rq, qo, qs + i * HD * 2, AUX_NT);
      if (i % 2 == 1 && i / 2 < 8) { const int t = i / 2; A[t] = __builtin_amdgcn_raw_buffer_load_b128(t < 4 ? rk : rv, vo + (t & 3) * 1024, so0, AUX_NT); }
      SB();
    }
    __builtin_amdgcn_s_setprio(0);
    float sum = 0.f;
#pragma unroll
    for (int i = 0; i < 4; ++i) sum += (bflo(xa[i]) + bfhi(xa[i])) + (bflo(xb[i]) + bfhi(xb[i]));
    const float mean = wave_sum(sum) * (1.f / D);
    float sq = 0.f, sq2 = 0.f;
#pragma unroll
    for (int i = 0; i < 4; ++i) {
      const float d0 = bflo(xa[i]) - mean, d1 = bfhi(xa[i]) - mean, d2 = bflo(xb[i]) - mean, d3 = bfhi(xb[i]) - mean;
      sq += d0 * d0 + d1 * d1; sq2 += d2 * d2 + d3 * d3;
    }
    sq += lane < 8 ? sq2 : 0.f;
    const float var = wave_sum(sq) * (1.f / D);
    const float mb = rbf(mean), den = rbf(sqrtf(rbf(rbf(var) + 1e-5f)));
    const unsigned xr[9] = {xr0.x, xr0.y, xr0.z, xr0.w, xr1.x, xr1.y, xr1.z, xr1.w, xr2};
    float qa[8];
#pragma unroll
    for (int i = 0; i < 8; ++i) qa[i] = 0.f;
#pragma unroll
    for (int p = 0; p < 9; ++p) {
      if (p == 4) { SB(); issue(Bf, K, V, lo + w + 8, true, vo); SB(); }
      const unsigned lp = cvtpk(ln1(bflo(xr[p]), mb, den), ln1(bfhi(xr[p]), mb, den));
#pragma unroll
      for (int d = 0; d < 4; ++d) {
        qa[2 * d] = dot2(__builtin_amdgcn_perm(wv[2 * p + 1][d], wv[2 * p][d], 0x05040100u), lp, qa[2 * d]);
        qa[2 * d + 1] = dot2(__builtin_amdgcn_perm(wv[2 * p + 1][d], wv[2 * p][d], 0x07060302u), lp, qa[2 * d + 1]);
      }
    }
    const float z0 = sw32_sum(qa[0], qa[4]), z1 = sw32_sum(qa[1], qa[5]), z2 = sw32_sum(qa[2], qa[6]), z3 = sw32_sum(qa[3], qa[7]);
    *(LAS f32x2*)(L + L_QP + (w * 128 + 8 * c + 2 * r) * 4) = f32x2{sw16_sum(z0, z2), sw16_sum(z1, z3)};
    STAMP(1);
    BAR();
    f32x4 q0 = {0.f, 0.f, 0.f, 0.f}, q1 = q0;
#pragma unroll
    for (int i = 0; i < 8; ++i) { q0 += *(LAS f32x4*)(L + L_QP + (i * 128 + 8 * c) * 4); q1 += *(LAS f32x4*)(L + L_QP + (i * 128 + 8 * c + 4) * 4); }
    qv = v4u{cvtpk(q0.x, q0.y), cvtpk(q0.z, q0.w), cvtpk(q1.x, q1.y), cvtpk(q1.z, q1.w)};
    STAMP(2);
  }
  float m = -INFINITY, l = 0.f, acc[8];
#pragma unroll
  for (int i = 0; i < 8; ++i) acc[i] = 0.f;
  for (int k = 0; k < NS - 2; k += 2) {
    if (k < 2 || k >= 2 + ne) tile(A, qv, m, l, acc);
    SB();
    issue(A, K, V, lo + w + 8 * (k + 2 - ne), k + 2 >= 2 + ne, vo);
    SB();
    asm volatile("s_barrier" ::: "memory");
    if (k + 1 < 2 || k + 1 >= 2 + ne) tile(Bf, qv, m, l, acc);
    SB();
    issue(Bf, K, V, lo + w + 8 * (k + 3 - ne), k + 3 >= 2 + ne, vo);
    SB();
    asm volatile("s_barrier" ::: "memory");
  }
  tile(A, qv, m, l, acc);
  tile(Bf, qv, m, l, acc);
  STAMP(3);
  const unsigned T = __builtin_amdgcn_readfirstlane(E) + 1u;
  {
    const float M = xrow_max(m), f = __builtin_amdgcn_exp2f(m - M);
    const float Lw = xrow_sum(l * f);
#pragma unroll
    for (int i = 0; i < 8; ++i) acc[i] *= f;
    const float z0 = sw32_sum(acc[0], acc[4]), z1 = sw32_sum(acc[1], acc[5]), z2 = sw32_sum(acc[2], acc[6]), z3 = sw32_sum(acc[3], acc[7]);
    *(LAS f32x2*)(L + L_QP + (w * 132 + 4 + 8 * c + 2 * r) * 4) = f32x2{sw16_sum(z0, z2), sw16_sum(z1, z3)};
    if (lane == 0) *(LAS f32x2*)(L + L_QP + (w * 132) * 4) = f32x2{M, Lw};
    BAR();
    if (w == 0) {
      f32x2 ml[8], o[8];
#pragma unroll
      for (int i = 0; i < 8; ++i) { ml[i] = *(LAS f32x2*)(L + L_QP + (i * 132) * 4); o[i] = *(LAS f32x2*)(L + L_QP + (i * 132 + 4 + 2 * lane) * 4); }
      float MM = ml[0].x;
#pragma unroll
      for (int i = 1; i < 8; ++i) MM = fmaxf(MM, ml[i].x);
      float LL = 0.f, o0 = 0.f, o1 = 0.f;
#pragma unroll
      for (int i = 0; i < 8; ++i) { const float wt = __builtin_amdgcn_exp2f(ml[i].x - MM); LL = fmaf(wt, ml[i].y, LL); o0 = fmaf(wt, o[i].x, o0); o1 = fmaf(wt, o[i].y, o1); }
      const int so = WS_S1 + ((h * 2 + b) * 4 + s) * S1_STR;
      __builtin_amdgcn_raw_buffer_store_b128(v4u{__float_as_uint(o0), T, __float_as_uint(o1), T}, rws, vo, so, AUX_SC1);
      if (lane == 0) __builtin_amdgcn_raw_buffer_store_b128(v4u{__float_as_uint(MM), T, __float_as_uint(LL), T}, rws, 1024, so, AUX_SC1);
      STAMP(4);
    }
    BAR();
  }
  const int q4 = c >> 2, p4 = c & 3;
  LAS unsigned char* priv = L + L_PRIV + w * 512;
  if (is_wo) {
    BAR();
    v4u bf[2][5];
#pragma unroll
    for (int ks = 0; ks < 2; ++ks)
#pragma unroll
      for (int t = 0; t < 5; ++t) { const unsigned a = L_W + (64 * w + 32 * ks + 8 * r + q4) * 144 + (16 * t + 4 * p4) * 2; bf[ks][t] = trfrag(L, a, a + 4 * 144); }
    const int pb = lane >> 5, base = ((4 * x8 + (w >> 1)) * 2 + pb) * 4 * S1_STR, cj = (32 * (w & 1) + (lane & 31)) * 16;
    v4u ov[4], mv[4];
#pragma nounroll
    for (unsigned spin = 0; spin < SPIN_CAP; ++spin) {
      asm volatile("" ::: "memory");
#pragma unroll
      for (int i = 0; i < 4; ++i) {
        ov[i] = __builtin_amdgcn_raw_buffer_load_b128(rws, base + i * S1_STR + cj, WS_S1, AUX_SC1);
        mv[i] = __builtin_amdgcn_raw_buffer_load_b128(rws, base + i * S1_STR + 1024, WS_S1, AUX_SC1);
      }
      unsigned bad = 0u;
#pragma unroll
      for (int i = 0; i < 4; ++i) bad |= BADC(ov[i]) | BADC(mv[i]);
      if (__all(bad == 0u)) break;
      __builtin_amdgcn_s_sleep(1);
    }
    STAMP(6);
    const float M = fmaxf(fmaxf(__uint_as_float(mv[0].x), __uint_as_float(mv[1].x)), fmaxf(__uint_as_float(mv[2].x), __uint_as_float(mv[3].x)));
    float LL = 0.f, o0 = 0.f, o1 = 0.f;
#pragma unroll
    for (int i = 0; i < 4; ++i) {
      const float wt = __builtin_amdgcn_exp2f(__uint_as_float(mv[i].x) - M);
      LL = fmaf(wt, __uint_as_float(mv[i].z), LL); o0 = fmaf(wt, __uint_as_float(ov[i].x), o0); o1 = fmaf(wt, __uint_as_float(ov[i].z), o1);
    }
    *(LAS unsigned*)(priv + lane * 4) = cvtpk(o0 / LL, o1 / LL);
    LWAIT();
    const v4u a0 = *(LAS v4u*)(priv + ((c & 1) * 32 + 4 * r) * 4), a1 = *(LAS v4u*)(priv + ((c & 1) * 32 + 16 + 4 * r) * 4);
#pragma unroll
    for (int t = 0; t < 5; ++t) {
      f32x4 d = mfma(a0, bf[0][t], f32x4{0.f, 0.f, 0.f, 0.f});
      d = mfma(a1, bf[1][t], d);
      if (lane < 16) *(LAS f32x2*)(L + L_RED + (w * 80 + 16 * t + lane) * 8) = f32x2{d.x, d.y};
    }
    BAR();
    if (tid < 72) {
      f32x2 y = {0.f, 0.f};
#pragma unroll
      for (int i = 0; i < 8; ++i) y += *(LAS f32x2*)(L + L_RED + (i * 80 + tid) * 8);
      __builtin_amdgcn_raw_buffer_store_b128(v4u{__float_as_uint(y.x), T, __float_as_uint(y.y), T}, rws, tid * 16, WS_S2 + (x8 * D + 72 * cg) * 16, AUX_SC1);
    }
    STAMP(7);
    return;
  }
  const int n1 = 72 * w + lane, n2 = 72 * w + 64 + (lane & 7);
  const float xa0 = bflo(x[n1]), xa1 = bflo(x[D + n1]), xb0 = bflo(x[n2]), xb1 = bflo(x[D + n2]);
  BAR();
  v4u gf[3][3], df[5];
#pragma unroll
  for (int i = 0; i < 3; ++i) {
    const int ks = (i < 2 || w < 2) ? w + 8 * i : 0;
#pragma unroll
    for (int t = 0; t < 3; ++t) {
      const int col = 16 * t + 4 * p4;
      const unsigned a = (col < 24 ? L_WG + col * 2 : L_WU + (col - 24) * 2) + (32 * ks + 8 * r + q4) * 48;
      gf[i][t] = trfrag(L, a, a + 4 * 48);
    }
  }
#pragma unroll
  for (int i = 0; i < 5; ++i) {
    const int t = (i < 4 || w < 4) ? w + 8 * i : 0;
    const unsigned a = L_WD + ((r < 3 ? 8 * r : 0) + q4) * 1152 + (16 * t + 4 * p4) * 2;
    const v4u f = trfrag(L, a, a + 4 * 1152);
    df[i] = r < 3 ? f : v4u{0u, 0u, 0u, 0u};
  }
  {
    v4u yv[8], ye;
#pragma nounroll
    for (unsigned spin = 0; spin < SPIN_CAP; ++spin) {
      asm volatile("" ::: "memory");
#pragma unroll
      for (int i = 0; i < 8; ++i) yv[i] = __builtin_amdgcn_raw_buffer_load_b128(rws, n1 * 16, WS_S2 + i * D * 16, AUX_SC1);
      ye = __builtin_amdgcn_raw_buffer_load_b128(rws, ((lane >> 3) * D + n2) * 16, WS_S2, AUX_SC1);
      unsigned bad = BADC(ye);
#pragma unroll
      for (int i = 0; i < 8; ++i) bad |= BADC(yv[i]);
      if (__all(bad == 0u)) break;
      __builtin_amdgcn_s_sleep(1);
    }
    STAMP(6);
    float y0 = 0.f, y1 = 0.f;
#pragma unroll
    for (int i = 0; i < 8; ++i) { y0 += __uint_as_float(yv[i].x); y1 += __uint_as_float(yv[i].z); }
    float e0 = __uint_as_float(ye.x), e1 = __uint_as_float(ye.z);
    e0 += dpp<0x128>(e0); e1 += dpp<0x128>(e1);
    e0 = xrow_sum(e0); e1 = xrow_sum(e1);
    *(LAS f32x2*)(L + L_H + n1 * 8) = f32x2{rbf(xa0 + rbf(y0)), rbf(xa1 + rbf(y1))};
    if (lane < 8) *(LAS f32x2*)(L + L_H + n2 * 8) = f32x2{rbf(xb0 + rbf(e0)), rbf(xb1 + rbf(e1))};
  }
  BAR();
  {
    f32x2 hv[9];
#pragma unroll
    for (int i = 0; i < 9; ++i) hv[i] = *(LAS f32x2*)(L + L_H + (64 * i + lane) * 8);
    float mine[3];
#pragma unroll
    for (int i = 0; i < 3; ++i) { const int ks = (i < 2 || w < 2) ? w + 8 * i : 0; mine[i] = *(LAS float*)(L + L_H + (32 * ks + (lane & 31)) * 8 + (lane >> 5) * 4); }
    f32x2 sm = hv[0];
#pragma unroll
    for (int i = 1; i < 9; ++i) sm += hv[i];
    const float m0 = wave_sum(sm.x) * (1.f / D), m1 = wave_sum(sm.y) * (1.f / D);
    float s0 = 0.f, s1 = 0.f;
#pragma unroll
    for (int i = 0; i < 9; ++i) { const float d0 = hv[i].x - m0, d1 = hv[i].y - m1; s0 = fmaf(d0, d0, s0); s1 = fmaf(d1, d1, s1); }
    const float v0 = wave_sum(s0) * (1.f / D), v1 = wave_sum(s1) * (1.f / D);
    const float mb = rbf(lane < 32 ? m0 : m1), den = rbf(sqrtf(rbf(rbf(lane < 32 ? v0 : v1) + 1e-5f)));
#pragma unroll
    for (int i = 0; i < 3; ++i) *(LAS u16*)(priv + i * 128 + lane * 2) = (u16)cvtpk(ln1(mine[i], mb, den), 0.f);
    LWAIT();
    f32x4 d[3];
#pragma unroll
    for (int t = 0; t < 3; ++t) d[t] = f32x4{0.f, 0.f, 0.f, 0.f};
#pragma unroll
    for (int i = 0; i < 3; ++i) {
      v4u a = *(LAS v4u*)(priv + i * 128 + (c & 1) * 64 + r * 16);
      if (i == 2 && w >= 2) a = v4u{0u, 0u, 0u, 0u};
#pragma unroll
      for (int t = 0; t < 3; ++t) d[t] = mfma(a, gf[i][t], d[t]);
    }
    if (lane < 16) {
#pragma unroll
      for (int t = 0; t < 3; ++t) *(LAS f32x2*)(L + L_RED + (w * 48 + 16 * t + lane) * 8) = f32x2{d[t].x, d[t].y};
    }
  }
  BAR();
  {
    const int pb = lane >> 5, cc = (lane & 31) < 24 ? (lane & 31) : 0;
    float gg = 0.f, uu = 0.f;
#pragma unroll
    for (int i = 0; i < 8; ++i) { gg += *(LAS float*)(L + L_RED + (i * 48 + cc) * 8 + pb * 4); uu += *(LAS float*)(L + L_RED + (i * 48 + 24 + cc) * 8 + pb * 4); }
    const float act = (lane & 31) < 24 ? rbf(gelu_tanh(rbf(gg))) * rbf(uu) : 0.f;
    *(LAS u16*)(priv + 384 + lane * 2) = (u16)cvtpk(act, 0.f);
    LWAIT();
    const v4u a = *(LAS v4u*)(priv + 384 + (c & 1) * 64 + r * 16);
#pragma unroll
    for (int i = 0; i < 5; ++i) {
      const f32x4 d = mfma(a, df[i], f32x4{0.f, 0.f, 0.f, 0.f});
      if (lane < 16 && (i < 4 || w < 4))
        __builtin_amdgcn_raw_buffer_store_b128(v4u{__float_as_uint(d.x), T, __float_as_uint(d.y), T}, rws, lane * 16, WS_S3 + (mi * D + 16 * (w + 8 * i)) * 16, AUX_SC1);
    }
  }
  STAMP(7);
  if (w == 7 && mi < 144) {
    const int n = 4 * mi + (lane & 3);
    const f32x2 hres = *(LAS f32x2*)(L + L_H + n * 8);
    v4u zv[12];
#pragma nounroll
    for (unsigned spin = 0; spin < SPIN_CAP; ++spin) {
      asm volatile("" ::: "memory");
#pragma unroll
      for (int i = 0; i < 12; ++i) zv[i] = __builtin_amdgcn_raw_buffer_load_b128(rws, ((lane >> 2) * D + n) * 16, WS_S3 + i * 16 * D * 16, AUX_SC1);
      unsigned bad = 0u;
#pragma unroll
      for (int i = 0; i < 12; ++i) bad |= BADC(zv[i]);
      if (__all(bad == 0u)) break;
    }
    STAMP(8);
    float z0 = 0.f, z1 = 0.f;
#pragma unroll
    for (int i = 0; i < 12; ++i) { z0 += __uint_as_float(zv[i].x); z1 += __uint_as_float(zv[i].z); }
    z0 += dpp<0x124>(z0); z1 += dpp<0x124>(z1); z0 += dpp<0x128>(z0); z1 += dpp<0x128>(z1);
    z0 = xrow_sum(z0); z1 = xrow_sum(z1);
    if (lane < 8) out[(lane >> 2) * D + n] = (u16)cvtpk(lane < 4 ? hres.x + rbf(z0) : hres.y + rbf(z1), 0.f);
    if (mi == 0 && lane == 0) __builtin_amdgcn_raw_buffer_store_b32(T, rws, 0, WS_E, AUX_SC1);
    STAMP(9);
  }
}

extern "C" void kernel_launch(void* const* d_in, const int* in_sizes, int n_in, void* d_out, int out_size, void* d_ws, size_t ws_size, hipStream_t stream) {
  (void)hipFuncSetAttribute((const void*)fused, hipFuncAttributeMaxDynamicSharedMemorySize, L_END);
  fused<<<NWG, NTHR, L_END, stream>>>((const u16*)d_in[0], (const u16*)d_in[1], (const u16*)d_in[2], (const u16*)d_in[3], (const u16*)d_in[4],
      (const u16*)d_in[6], (const u16*)d_in[7], (const u16*)d_in[8], (u16*)d_out, (unsigned char*)d_ws
      , (unsigned long long*)((char*)d_ws + (64u << 20))
      );
  hipStreamCaptureStatus cs; (void)hipStreamIsCapturing(stream, &cs);
  if (cs == hipStreamCaptureStatusNone) {
    for (int rep = 0; rep < 4; ++rep) {
    (void)hipStreamSynchronize(stream);
    for (int i = 0; i < 9 && rep == 0; ++i) { hipPointerAttribute_t at; (void)hipPointerGetAttributes(&at, d_in[i]); void* base; size_t sz; (void)hipMemGetAddressRange((hipDeviceptr_t*)&base, &sz, d_in[i]);
      fprintf(stderr, "in%d %p type %d flags %x range base %p size %zu\n", i, d_in[i], (int)at.type, at.allocationFlags, base, sz); }
    if (rep == 0) { hipPointerAttribute_t at; (void)hipPointerGetAttributes(&at, d_ws); void* base; size_t sz; (void)hipMemGetAddressRange((hipDeviceptr_t*)&base, &sz, d_ws); fprintf(stderr, "ws %p type %d flags %x base %p size %zu out %p\n", d_ws, (int)at.type, at.allocationFlags, base, sz, d_out); }
    std::vector<unsigned long long> st(256 * 9 * 16);
    (void)hipMemcpy(st.data(), (char*)d_ws + (64u << 20), st.size() * 8, hipMemcpyDeviceToHost);
    unsigned long long t0 = ~0ull; for (int i = 0; i < 256 * 9; ++i) t0 = std::min(t0, st[i * 16]);
    for (int k = 0; k < 10; ++k) { std::vector<double> v; for (int i = 0; i < 256 * 9; ++i) if (st[i * 16 + k] >= t0 && st[i * 16 + k] < t0 + 100000) v.push_back((st[i * 16 + k] - t0) * 0.01);
      if (v.empty()) continue; std::sort(v.begin(), v.end()); fprintf(stderr, "stamp %d: min %.2f med %.2f max %.2f\n", k, v.front(), v[v.size() / 2], v.back()); }
    (void)hipMemsetAsync((char*)d_ws + (128u << 20), 0x5A, ws_size - (128u << 20), stream);
    (void)hipStreamSynchronize(stream);
    fused<<<NWG, NTHR, L_END, stream>>>((const u16*)d_in[0], (const u16*)d_in[1], (const u16*)d_in[2], (const u16*)d_in[3], (const u16*)d_in[4],
      (const u16*)d_in[6], (const u16*)d_in[7], (const u16*)d_in[8], (u16*)d_out, (unsigned char*)d_ws, (unsigned long long*)((char*)d_ws + (64u << 20)));
    }
  }
}
```

```cpp
#include <hip/hip_runtime.h>
#include <hip/hip_bf16.h>
#include <stdint.h>

typedef unsigned short u16;
typedef unsigned v4u __attribute__((ext_vector_type(4)));
typedef unsigned v2u __attribute__((ext_vector_type(2)));
typedef float f32x4 __attribute__((ext_vector_type(4)));
typedef float f32x2 __attribute__((ext_vector_type(2)));
typedef short s16x4 __attribute__((ext_vector_type(4)));
typedef __bf16 bf16x2 __attribute__((ext_vector_type(2)));
typedef __bf16 bf16x8 __attribute__((ext_vector_type(8)));
#define LAS __attribute__((address_space(3)))
#define GAS __attribute__((address_space(1)))

constexpr int D = 576, H = 32, SK = 8192, DH = 128, HD = H * DH, DF = 4608;
constexpr int NWG = 256, NTHR = 576;
constexpr int T_WO = 122, T_MLP = 130;
#define NPRE 32
constexpr unsigned WS_E = 0, WS_S1 = 4096, S1_STR = 1152, WS_S2 = WS_S1 + 256 * S1_STR, WS_S3 = WS_S2 + 8 * D * 16, WS_END = WS_S3 + 192 * D * 16;
constexpr unsigned L_W = 0, L_WG = 0, L_WU = 27648, L_WD = 55296, L_WEND = 82944 + 256;
constexpr unsigned L_QP = L_WEND;
constexpr unsigned L_H = L_QP + 8 * 132 * 4;
constexpr unsigned L_RED = L_H + D * 8;
constexpr unsigned L_PRIV = L_RED + 8 * 80 * 8;
constexpr unsigned L_END = L_PRIV + 8 * 512;
constexpr int AUX_NT = 2, AUX_SC1 = 16, RS3 = 0x00020000;
constexpr unsigned SPIN_CAP = 1u << 17;

#define STAMP(i) do {} while (0)

__device__ __forceinline__ float bflo(unsigned u) { return __uint_as_float(u << 16); }
__device__ __forceinline__ float bfhi(unsigned u) { return __uint_as_float(u & 0xFFFF0000u); }
__device__ __forceinline__ unsigned cvtpk(float lo, float hi) {
  const bf16x2 r = {(__bf16)lo, (__bf16)hi};
  return __builtin_bit_cast(unsigned, r);
}
__device__ __forceinline__ float rbf(float f) { return bflo(cvtpk(f, f)); }
__device__ __forceinline__ float dot2(unsigned a, unsigned b, float c) {
  return __builtin_amdgcn_fdot2_f32_bf16(__builtin_bit_cast(bf16x2, a), __builtin_bit_cast(bf16x2, b), c, false);
}
template <int CTRL> __device__ __forceinline__ float dpp(float x) {
  return __builtin_bit_cast(float, __builtin_amdgcn_mov_dpp(__builtin_bit_cast(int, x), CTRL, 0xf, 0xf, true));
}
__device__ __forceinline__ float row_sum(float d) { d += dpp<0x128>(d); d += dpp<0x124>(d); d += dpp<0x122>(d); d += dpp<0x121>(d); return d; }
__device__ __forceinline__ float sw32_sum(float a, float b) {
  auto t = __builtin_amdgcn_permlane32_swap(__float_as_uint(a), __float_as_uint(b), false, false);
  return __uint_as_float(t[0]) + __uint_as_float(t[1]);
}
__device__ __forceinline__ float sw16_sum(float a, float b) {
  auto t = __builtin_amdgcn_permlane16_swap(__float_as_uint(a), __float_as_uint(b), false, false);
  return __uint_as_float(t[0]) + __uint_as_float(t[1]);
}
__device__ __forceinline__ float xrow_sum(float x) { x = sw16_sum(x, x); return sw32_sum(x, x); }
__device__ __forceinline__ float xrow_max(float x) {
  auto s = __builtin_amdgcn_permlane16_swap(__float_as_uint(x), __float_as_uint(x), false, false);
  x = fmaxf(__uint_as_float(s[0]), __uint_as_float(s[1]));
  auto t = __builtin_amdgcn_permlane32_swap(__float_as_uint(x), __float_as_uint(x), false, false);
  return fmaxf(__uint_as_float(t[0]), __uint_as_float(t[1]));
}
__device__ __forceinline__ float wave_sum(float x) { return xrow_sum(row_sum(x)); }
__device__ __forceinline__ f32x4 mfma(v4u a, v4u b, f32x4 c) {
  return __builtin_amdgcn_mfma_f32_16x16x32_bf16(__builtin_bit_cast(bf16x8, a), __builtin_bit_cast(bf16x8, b), c, 0, 0, 0);
}
__device__ __forceinline__ v4u trfrag(LAS unsigned char* L, unsigned a0, unsigned a1) {
  const s16x4 x = __builtin_amdgcn_ds_read_tr16_b64_v4i16((LAS s16x4*)(L + a0));
  const s16x4 y = __builtin_amdgcn_ds_read_tr16_b64_v4i16((LAS s16x4*)(L + a1));
  const v2u p = __builtin_bit_cast(v2u, x), q = __builtin_bit_cast(v2u, y);
  return v4u{p.x, p.y, q.x, q.y};
}
#define BAR() asm volatile("s_waitcnt lgkmcnt(0)\n\ts_barrier" ::: "memory")
#define LWAIT() asm volatile("s_waitcnt lgkmcnt(0)" ::: "memory")
#define SB() __builtin_amdgcn_sched_barrier(0)
#define BADC(v) (((v).y ^ T) | ((v).w ^ T))
constexpr float L2E = 1.4426950408889634f, SCALE = 0.08838834764831845f;

__device__ __forceinline__ void tile(const v4u (&kv)[8], const v4u qv, float& m, float& l, float (&acc)[8]) {
  float d[4];
#pragma unroll
  for (int i = 0; i < 4; ++i) {
    float t = dot2(kv[i].x, qv.x, 0.f); t = dot2(kv[i].y, qv.y, t); t = dot2(kv[i].z, qv.z, t); t = dot2(kv[i].w, qv.w, t);
    d[i] = row_sum(t);
  }
  unsigned p01 = cvtpk(d[0], d[1]), p23 = cvtpk(d[2], d[3]);
  p01 = cvtpk(bflo(p01) * SCALE, bfhi(p01) * SCALE); p23 = cvtpk(bflo(p23) * SCALE, bfhi(p23) * SCALE);
  const float s[4] = {bflo(p01) * L2E, bfhi(p01) * L2E, bflo(p23) * L2E, bfhi(p23) * L2E};
  const float mn = fmaxf(fmaxf(fmaxf(m, s[0]), s[1]), fmaxf(s[2], s[3]));
  const float al = __builtin_amdgcn_exp2f(m - mn);
  m = mn; l *= al;
#pragma unroll
  for (int j = 0; j < 8; ++j) acc[j] *= al;
#pragma unroll
  for (int i = 0; i < 4; ++i) {
    const float p = __builtin_amdgcn_exp2f(s[i] - mn);
    l += p;
#pragma unroll
    for (int j = 0; j < 4; ++j) {
      acc[2 * j] = fmaf(p, bflo(kv[4 + i][j]), acc[2 * j]);
      acc[2 * j + 1] = fmaf(p, bfhi(kv[4 + i][j]), acc[2 * j + 1]);
    }
  }
}
__device__ __forceinline__ void issue(v4u (&kv)[8], const u16* K, const u16* V, int tile_idx, bool live, int vo) {
  const int nrec = live ? (int)(128u << 20) : 0;
  const __amdgpu_buffer_rsrc_t rk = __builtin_amdgcn_make_buffer_rsrc((void*)K, 0, nrec, RS3);
  const __amdgpu_buffer_rsrc_t rv = __builtin_amdgcn_make_buffer_rsrc((void*)V, 0, nrec, RS3);
  const int so = live ? tile_idx * 4096 : 0;
#pragma unroll
  for (int i = 0; i < 4; ++i) kv[i] = __builtin_amdgcn_raw_buffer_load_b128(rk, vo + i * 1024, so, 0);
#pragma unroll
  for (int i = 0; i < 4; ++i) kv[4 + i] = __builtin_amdgcn_raw_buffer_load_b128(rv, vo + i * 1024, so, AUX_NT);
}
__device__ __forceinline__ float ln1(float v, float mb, float den) { return rbf(v - mb) / den; }
__device__ __forceinline__ float gelu_tanh(float x) {
  const float k = 0.7978845608028654f * (x + 0.044715f * x * x * x);
  const float e = __builtin_amdgcn_exp2f(k * (2.f * L2E));
  const float th = 1.f - 2.f / (e + 1.f);
  return 0.5f * x * (1.f + th);
}

__global__ void __launch_bounds__(NTHR, 3) fused(const u16* __restrict__ x, const u16* __restrict__ K, const u16* __restrict__ V, const u16* __restrict__ wq,
    const u16* __restrict__ wo, const u16* __restrict__ wg, const u16* __restrict__ wu, const u16* __restrict__ wd, u16* __restrict__ out, unsigned char* ws
    ) {
  extern __shared__ __attribute__((aligned(16))) unsigned char smem[];
  LAS unsigned char* L = (LAS unsigned char*)smem;
  const int tid = threadIdx.x, lane = tid & 63, w = __builtin_amdgcn_readfirstlane(tid >> 6), r = lane >> 4, c = lane & 15;
  const int g = blockIdx.x, x8 = g & 7, j = g >> 3, h = x8 * 4 + (j >> 3), b = (j >> 2) & 1, s = j & 3;
  const bool is_wo = (s == 0);
  const int cg = j >> 2, mi = x8 * 24 + (j >> 2) * 3 + (s - 1);
  const int NS = is_wo ? 16 : 18;
  STAMP(0);
  if (w == 8) {
    if (is_wo) {
      const unsigned char* src = (const unsigned char*)wo + ((size_t)(512 * x8 + lane / 9) * D + 72 * cg + 8 * (lane % 9)) * 2;
      int cc = lane % 9, p = 0;
      BAR();
      for (int k = 0; k < NS - 2; ++k) {
        for (int e = 0; e < 6 && p < 72; ++e, ++p) {
          __builtin_amdgcn_global_load_lds((const GAS void*)src, (LAS void*)(L + L_W + p * 1024), 16, 0, AUX_NT);
          src += 7 * 1152 + 16; if (++cc == 9) { cc = 0; src += 1152 - 144; }
        }
        asm volatile("s_barrier" ::: "memory");
      }
      asm volatile("s_waitcnt vmcnt(0)" ::: "memory");
      BAR(); BAR(); BAR(); BAR();
    } else {
      const unsigned goff = ((unsigned)(lane / 3) * DF + 24 * mi + 8 * (lane % 3)) * 2;
      const unsigned char* sg = (const unsigned char*)wg + goff;
      const unsigned char* su = (const unsigned char*)wu + goff;
      const unsigned char* sd = (const unsigned char*)wd + (size_t)24 * mi * D * 2 + lane * 16;
      int cc = lane % 3;
      BAR();
      for (int k = 0; k < NS - 2; ++k) asm volatile("s_barrier" ::: "memory");
      for (int p = 0; p < 27; ++p) {
        if (3 * p == (NPRE / 3) * 3) { BAR(); BAR(); }
        __builtin_amdgcn_global_load_lds((const GAS void*)sg, (LAS void*)(L + L_WG + p * 1024), 16, 0, AUX_NT);
        __builtin_amdgcn_global_load_lds((const GAS void*)su, (LAS void*)(L + L_WU + p * 1024), 16, 0, AUX_NT);
        __builtin_amdgcn_global_load_lds((const GAS void*)sd, (LAS void*)(L + L_WD + p * 1024), 16, 0, AUX_NT);
        int adv = 21 * 9216 + 16; if (++cc == 3) { cc = 0; adv += 9216 - 48; }
        sg += adv; su += adv; sd += 1024;
      }
      if (NPRE / 3 >= 27) { BAR(); BAR(); }
      asm volatile("s_waitcnt vmcnt(0)" ::: "memory");
      STAMP(5);
      BAR(); BAR(); BAR();
    }
    return;
  }
  const __amdgpu_buffer_rsrc_t rws = __builtin_amdgcn_make_buffer_rsrc((void*)ws, 0, (int)WS_END, RS3);
  const unsigned E = __builtin_amdgcn_raw_buffer_load_b32(rws, 0, WS_E, AUX_SC1);
  const int lo = (b * H + h) * 512 + (is_wo ? 0 : T_WO + (s - 1) * T_MLP), ntl = is_wo ? T_WO : T_MLP;
  const int ne = NS - ((ntl - w + 7) >> 3);
  const int vo = lane * 16;
  v4u A[8], Bf[8];
  v4u qv;
  {
    const __amdgpu_buffer_rsrc_t rx = __builtin_amdgcn_make_buffer_rsrc((void*)(x + b * D), 0, D * 2, RS3);
    const v4u xa = __builtin_amdgcn_raw_buffer_load_b128(rx, vo, 0, 0), xb = __builtin_amdgcn_raw_buffer_load_b128(rx, vo + 1024, 0, 0);
    const int xo = (72 * w + 18 * r) * 2;
    const v4u xr0 = __builtin_amdgcn_raw_buffer_load_b128(rx, xo, 0, 0), xr1 = __builtin_amdgcn_raw_buffer_load_b128(rx, xo + 16, 0, 0);
    const unsigned xr2 = __builtin_amdgcn_raw_buffer_load_b32(rx, xo + 32, 0, 0);
    const __amdgpu_buffer_rsrc_t rq = __builtin_amdgcn_make_buffer_rsrc((void*)wq, 0, D * HD * 2, RS3);
    const __amdgpu_buffer_rsrc_t rk = __builtin_amdgcn_make_buffer_rsrc((void*)K, 0, (int)(128u << 20), RS3);
    const __amdgpu_buffer_rsrc_t rv = __builtin_amdgcn_make_buffer_rsrc((void*)V, 0, (int)(128u << 20), RS3);
    const int qo = (18 * r * HD + 8 * c) * 2, qs = (72 * w * HD + h * DH) * 2, so0 = (lo + w) * 4096;
    v4u wv[18];
    __builtin_amdgcn_s_setprio(3);
#pragma unroll
    for (int i = 0; i < 18; ++i) {
      wv[i] = __builtin_amdgcn_raw_buffer_load_b128(rq, qo, qs + i * HD * 2, AUX_NT);
      if (i % 2 == 1 && i / 2 < 8) { const int t = i / 2; A[t] = __builtin_amdgcn_raw_buffer_load_b128(t < 4 ? rk : rv, vo + (t & 3) * 1024, so0, AUX_NT); }
      SB();
    }
    __builtin_amdgcn_s_setprio(0);
    float sum = 0.f;
#pragma unroll
    for (int i = 0; i < 4; ++i) sum += (bflo(xa[i]) + bfhi(xa[i])) + (bflo(xb[i]) + bfhi(xb[i]));
    const float mean = wave_sum(sum) * (1.f / D);
    float sq = 0.f, sq2 = 0.f;
#pragma unroll
    for (int i = 0; i < 4; ++i) {
      const float d0 = bflo(xa[i]) - mean, d1 = bfhi(xa[i]) - mean, d2 = bflo(xb[i]) - mean, d3 = bfhi(xb[i]) - mean;
      sq += d0 * d0 + d1 * d1; sq2 += d2 * d2 + d3 * d3;
    }
    sq += lane < 8 ? sq2 : 0.f;
    const float var = wave_sum(sq) * (1.f / D);
    const float mb = rbf(mean), den = rbf(sqrtf(rbf(rbf(var) + 1e-5f)));
    const unsigned xr[9] = {xr0.x, xr0.y, xr0.z, xr0.w, xr1.x, xr1.y, xr1.z, xr1.w, xr2};
    float qa[8];
#pragma unroll
    for (int i = 0; i < 8; ++i) qa[i] = 0.f;
#pragma unroll
    for (int p = 0; p < 9; ++p) {
      if (p == 4) { SB(); issue(Bf, K, V, lo + w + 8, true, vo); SB(); }
      const unsigned lp = cvtpk(ln1(bflo(xr[p]), mb, den), ln1(bfhi(xr[p]), mb, den));
#pragma unroll
      for (int d = 0; d < 4; ++d) {
        qa[2 * d] = dot2(__builtin_amdgcn_perm(wv[2 * p + 1][d], wv[2 * p][d], 0x05040100u), lp, qa[2 * d]);
        qa[2 * d + 1] = dot2(__builtin_amdgcn_perm(wv[2 * p + 1][d], wv[2 * p][d], 0x07060302u), lp, qa[2 * d + 1]);
      }
    }
    const float z0 = sw32_sum(qa[0], qa[4]), z1 = sw32_sum(qa[1], qa[5]), z2 = sw32_sum(qa[2], qa[6]), z3 = sw32_sum(qa[3], qa[7]);
    *(LAS f32x2*)(L + L_QP + (w * 128 + 8 * c + 2 * r) * 4) = f32x2{sw16_sum(z0, z2), sw16_sum(z1, z3)};
    STAMP(1);
    BAR();
    f32x4 q0 = {0.f, 0.f, 0.f, 0.f}, q1 = q0;
#pragma unroll
    for (int i = 0; i < 8; ++i) { q0 += *(LAS f32x4*)(L + L_QP + (i * 128 + 8 * c) * 4); q1 += *(LAS f32x4*)(L + L_QP + (i * 128 + 8 * c + 4) * 4); }
    qv = v4u{cvtpk(q0.x, q0.y), cvtpk(q0.z, q0.w), cvtpk(q1.x, q1.y), cvtpk(q1.z, q1.w)};
    STAMP(2);
  }
  float m = -INFINITY, l = 0.f, acc[8];
#pragma unroll
  for (int i = 0; i < 8; ++i) acc[i] = 0.f;
  for (int k = 0; k < NS - 2; k += 2) {
    if (k < 2 || k >= 2 + ne) tile(A, qv, m, l, acc);
    SB();
    issue(A, K, V, lo + w + 8 * (k + 2 - ne), k + 2 >= 2 + ne, vo);
    SB();
    asm volatile("s_barrier" ::: "memory");
    if (k + 1 < 2 || k + 1 >= 2 + ne) tile(Bf, qv, m, l, acc);
    SB();
    issue(Bf, K, V, lo + w + 8 * (k + 3 - ne), k + 3 >= 2 + ne, vo);
    SB();
    asm volatile("s_barrier" ::: "memory");
  }
  tile(A, qv, m, l, acc);
  tile(Bf, qv, m, l, acc);
  STAMP(3);
  const unsigned T = __builtin_amdgcn_readfirstlane(E) + 1u;
  {
    const float M = xrow_max(m), f = __builtin_amdgcn_exp2f(m - M);
    const float Lw = xrow_sum(l * f);
#pragma unroll
    for (int i = 0; i < 8; ++i) acc[i] *= f;
    const float z0 = sw32_sum(acc[0], acc[4]), z1 = sw32_sum(acc[1], acc[5]), z2 = sw32_sum(acc[2], acc[6]), z3 = sw32_sum(acc[3], acc[7]);
    *(LAS f32x2*)(L + L_QP + (w * 132 + 4 + 8 * c + 2 * r) * 4) = f32x2{sw16_sum(z0, z2), sw16_sum(z1, z3)};
    if (lane == 0) *(LAS f32x2*)(L + L_QP + (w * 132) * 4) = f32x2{M, Lw};
    BAR();
    if (w == 0) {
      f32x2 ml[8], o[8];
#pragma unroll
      for (int i = 0; i < 8; ++i) { ml[i] = *(LAS f32x2*)(L + L_QP + (i * 132) * 4); o[i] = *(LAS f32x2*)(L + L_QP + (i * 132 + 4 + 2 * lane) * 4); }
      float MM = ml[0].x;
#pragma unroll
      for (int i = 1; i < 8; ++i) MM = fmaxf(MM, ml[i].x);
      float LL = 0.f, o0 = 0.f, o1 = 0.f;
#pragma unroll
      for (int i = 0; i < 8; ++i) { const float wt = __builtin_amdgcn_exp2f(ml[i].x - MM); LL = fmaf(wt, ml[i].y, LL); o0 = fmaf(wt, o[i].x, o0); o1 = fmaf(wt, o[i].y, o1); }
      const int so = WS_S1 + ((h * 2 + b) * 4 + s) * S1_STR;
      __builtin_amdgcn_raw_buffer_store_b128(v4u{__float_as_uint(o0), T, __float_as_uint(o1), T}, rws, vo, so, AUX_SC1);
      if (lane == 0) __builtin_amdgcn_raw_buffer_store_b128(v4u{__float_as_uint(MM), T, __float_as_uint(LL), T}, rws, 1024, so, AUX_SC1);
      STAMP(4);
    }
    BAR();
  }
  const int q4 = c >> 2, p4 = c & 3;
  LAS unsigned char* priv = L + L_PRIV + w * 512;
  if (is_wo) {
    BAR();
    v4u bf[2][5];
#pragma unroll
    for (int ks = 0; ks < 2; ++ks)
#pragma unroll
      for (int t = 0; t < 5; ++t) { const unsigned a = L_W + (64 * w + 32 * ks + 8 * r + q4) * 144 + (16 * t + 4 * p4) * 2; bf[ks][t] = trfrag(L, a, a + 4 * 144); }
    const int pb = lane >> 5, base = ((4 * x8 + (w >> 1)) * 2 + pb) * 4 * S1_STR, cj = (32 * (w & 1) + (lane & 31)) * 16;
    v4u ov[4], mv[4];
#pragma nounroll
    for (unsigned spin = 0; spin < SPIN_CAP; ++spin) {
      asm volatile("" ::: "memory");
#pragma unroll
      for (int i = 0; i < 4; ++i) {
        ov[i] = __builtin_amdgcn_raw_buffer_load_b128(rws, base + i * S1_STR + cj, WS_S1, AUX_SC1);
        mv[i] = __builtin_amdgcn_raw_buffer_load_b128(rws, base + i * S1_STR + 1024, WS_S1, AUX_SC1);
      }
      unsigned bad = 0u;
#pragma unroll
      for (int i = 0; i < 4; ++i) bad |= BADC(ov[i]) | BADC(mv[i]);
      if (__all(bad == 0u)) break;
      __builtin_amdgcn_s_sleep(1);
    }
    STAMP(6);
    const float M = fmaxf(fmaxf(__uint_as_float(mv[0].x), __uint_as_float(mv[1].x)), fmaxf(__uint_as_float(mv[2].x), __uint_as_float(mv[3].x)));
    float LL = 0.f, o0 = 0.f, o1 = 0.f;
#pragma unroll
    for (int i = 0; i < 4; ++i) {
      const float wt = __builtin_amdgcn_exp2f(__uint_as_float(mv[i].x) - M);
      LL = fmaf(wt, __uint_as_float(mv[i].z), LL); o0 = fmaf(wt, __uint_as_float(ov[i].x), o0); o1 = fmaf(wt, __uint_as_float(ov[i].z), o1);
    }
    *(LAS unsigned*)(priv + lane * 4) = cvtpk(o0 / LL, o1 / LL);
    LWAIT();
    const v4u a0 = *(LAS v4u*)(priv + ((c & 1) * 32 + 4 * r) * 4), a1 = *(LAS v4u*)(priv + ((c & 1) * 32 + 16 + 4 * r) * 4);
#pragma unroll
    for (int t = 0; t < 5; ++t) {
      f32x4 d = mfma(a0, bf[0][t], f32x4{0.f, 0.f, 0.f, 0.f});
      d = mfma(a1, bf[1][t], d);
      if (lane < 16) *(LAS f32x2*)(L + L_RED + (w * 80 + 16 * t + lane) * 8) = f32x2{d.x, d.y};
    }
    BAR();
    if (tid < 72) {
      f32x2 y = {0.f, 0.f};
#pragma unroll
      for (int i = 0; i < 8; ++i) y += *(LAS f32x2*)(L + L_RED + (i * 80 + tid) * 8);
      __builtin_amdgcn_raw_buffer_store_b128(v4u{__float_as_uint(y.x), T, __float_as_uint(y.y), T}, rws, tid * 16, WS_S2 + (x8 * D + 72 * cg) * 16, AUX_SC1);
    }
    STAMP(7);
    return;
  }
  const int n1 = 72 * w + lane, n2 = 72 * w + 64 + (lane & 7);
  const float xa0 = bflo(x[n1]), xa1 = bflo(x[D + n1]), xb0 = bflo(x[n2]), xb1 = bflo(x[D + n2]);
  BAR();
  v4u gf[3][3], df[5];
#pragma unroll
  for (int i = 0; i < 3; ++i) {
    const int ks = (i < 2 || w < 2) ? w + 8 * i : 0;
#pragma unroll
    for (int t = 0; t < 3; ++t) {
      const int col = 16 * t + 4 * p4;
      const unsigned a = (col < 24 ? L_WG + col * 2 : L_WU + (col - 24) * 2) + (32 * ks + 8 * r + q4) * 48;
      gf[i][t] = trfrag(L, a, a + 4 * 48);
    }
  }
#pragma unroll
  for (int i = 0; i < 5; ++i) {
    const int t = (i < 4 || w < 4) ? w + 8 * i : 0;
    const unsigned a = L_WD + ((r < 3 ? 8 * r : 0) + q4) * 1152 + (16 * t + 4 * p4) * 2;
    const v4u f = trfrag(L, a, a + 4 * 1152);
    df[i] = r < 3 ? f : v4u{0u, 0u, 0u, 0u};
  }
  {
    v4u yv[8], ye;
#pragma nounroll
    for (unsigned spin = 0; spin < SPIN_CAP; ++spin) {
      asm volatile("" ::: "memory");
#pragma unroll
      for (int i = 0; i < 8; ++i) yv[i] = __builtin_amdgcn_raw_buffer_load_b128(rws, n1 * 16, WS_S2 + i * D * 16, AUX_SC1);
      ye = __builtin_amdgcn_raw_buffer_load_b128(rws, ((lane >> 3) * D + n2) * 16, WS_S2, AUX_SC1);
      unsigned bad = BADC(ye);
#pragma unroll
      for (int i = 0; i < 8; ++i) bad |= BADC(yv[i]);
      if (__all(bad == 0u)) break;
      __builtin_amdgcn_s_sleep(1);
    }
    STAMP(6);
    float y0 = 0.f, y1 = 0.f;
#pragma unroll
    for (int i = 0; i < 8; ++i) { y0 += __uint_as_float(yv[i].x); y1 += __uint_as_float(yv[i].z); }
    float e0 = __uint_as_float(ye.x), e1 = __uint_as_float(ye.z);
    e0 += dpp<0x128>(e0); e1 += dpp<0x128>(e1);
    e0 = xrow_sum(e0); e1 = xrow_sum(e1);
    *(LAS f32x2*)(L + L_H + n1 * 8) = f32x2{rbf(xa0 + rbf(y0)), rbf(xa1 + rbf(y1))};
    if (lane < 8) *(LAS f32x2*)(L + L_H + n2 * 8) = f32x2{rbf(xb0 + rbf(e0)), rbf(xb1 + rbf(e1))};
  }
  BAR();
  {
    f32x2 hv[9];
#pragma unroll
    for (int i = 0; i < 9; ++i) hv[i] = *(LAS f32x2*)(L + L_H + (64 * i + lane) * 8);
    float mine[3];
#pragma unroll
    for (int i = 0; i < 3; ++i) { const int ks = (i < 2 || w < 2) ? w + 8 * i : 0; mine[i] = *(LAS float*)(L + L_H + (32 * ks + (lane & 31)) * 8 + (lane >> 5) * 4); }
    f32x2 sm = hv[0];
#pragma unroll
    for (int i = 1; i < 9; ++i) sm += hv[i];
    const float m0 = wave_sum(sm.x) * (1.f / D), m1 = wave_sum(sm.y) * (1.f / D);
    float s0 = 0.f, s1 = 0.f;
#pragma unroll
    for (int i = 0; i < 9; ++i) { const float d0 = hv[i].x - m0, d1 = hv[i].y - m1; s0 = fmaf(d0, d0, s0); s1 = fmaf(d1, d1, s1); }
    const float v0 = wave_sum(s0) * (1.f / D), v1 = wave_sum(s1) * (1.f / D);
    const float mb = rbf(lane < 32 ? m0 : m1), den = rbf(sqrtf(rbf(rbf(lane < 32 ? v0 : v1) + 1e-5f)));
#pragma unroll
    for (int i = 0; i < 3; ++i) *(LAS u16*)(priv + i * 128 + lane * 2) = (u16)cvtpk(ln1(mine[i], mb, den), 0.f);
    LWAIT();
    f32x4 d[3];
#pragma unroll
    for (int t = 0; t < 3; ++t) d[t] = f32x4{0.f, 0.f, 0.f, 0.f};
#pragma unroll
    for (int i = 0; i < 3; ++i) {
      v4u a = *(LAS v4u*)(priv + i * 128 + (c & 1) * 64 + r * 16);
      if (i == 2 && w >= 2) a = v4u{0u, 0u, 0u, 0u};
#pragma unroll
      for (int t = 0; t < 3; ++t) d[t] = mfma(a, gf[i][t], d[t]);
    }
    if (lane < 16) {
#pragma unroll
      for (int t = 0; t < 3; ++t) *(LAS f32x2*)(L + L_RED + (w * 48 + 16 * t + lane) * 8) = f32x2{d[t].x, d[t].y};
    }
  }
  BAR();
  {
    const int pb = lane >> 5, cc = (lane & 31) < 24 ? (lane & 31) : 0;
    float gg = 0.f, uu = 0.f;
#pragma unroll
    for (int i = 0; i < 8; ++i) { gg += *(LAS float*)(L + L_RED + (i * 48 + cc) * 8 + pb * 4); uu += *(LAS float*)(L + L_RED + (i * 48 + 24 + cc) * 8 + pb * 4); }
    const float act = (lane & 31) < 24 ? rbf(gelu_tanh(rbf(gg))) * rbf(uu) : 0.f;
    *(LAS u16*)(priv + 384 + lane * 2) = (u16)cvtpk(act, 0.f);
    LWAIT();
    const v4u a = *(LAS v4u*)(priv + 384 + (c & 1) * 64 + r * 16);
#pragma unroll
    for (int i = 0; i < 5; ++i) {
      const f32x4 d = mfma(a, df[i], f32x4{0.f, 0.f, 0.f, 0.f});
      if (lane < 16 && (i < 4 || w < 4))
        __builtin_amdgcn_raw_buffer_store_b128(v4u{__float_as_uint(d.x), T, __float_as_uint(d.y), T}, rws, lane * 16, WS_S3 + (mi * D + 16 * (w + 8 * i)) * 16, AUX_SC1);
    }
  }
  STAMP(7);
  if (w == 7 && mi < 144) {
    const int n = 4 * mi + (lane & 3);
    const f32x2 hres = *(LAS f32x2*)(L + L_H + n * 8);
    v4u zv[12];
#pragma nounroll
    for (unsigned spin = 0; spin < SPIN_CAP; ++spin) {
      asm volatile("" ::: "memory");
#pragma unroll
      for (int i = 0; i < 12; ++i) zv[i] = __builtin_amdgcn_raw_buffer_load_b128(rws, ((lane >> 2) * D + n) * 16, WS_S3 + i * 16 * D * 16, AUX_SC1);
      unsigned bad = 0u;
#pragma unroll
      for (int i = 0; i < 12; ++i) bad |= BADC(zv[i]);
      if (__all(bad == 0u)) break;
    }
    STAMP(8);
    float z0 = 0.f, z1 = 0.f;
#pragma unroll
    for (int i = 0; i < 12; ++i) { z0 += __uint_as_float(zv[i].x); z1 += __uint_as_float(zv[i].z); }
    z0 += dpp<0x124>(z0); z1 += dpp<0x124>(z1); z0 += dpp<0x128>(z0); z1 += dpp<0x128>(z1);
    z0 = xrow_sum(z0); z1 = xrow_sum(z1);
    if (lane < 8) out[(lane >> 2) * D + n] = (u16)cvtpk(lane < 4 ? hres.x + rbf(z0) : hres.y + rbf(z1), 0.f);
    if (mi == 0 && lane == 0) __builtin_amdgcn_raw_buffer_store_b32(T, rws, 0, WS_E, AUX_SC1);
    STAMP(9);
  }
}

extern "C" void kernel_launch(void* const* d_in, const int* in_sizes, int n_in, void* d_out, int out_size, void* d_ws, size_t ws_size, hipStream_t stream) {
  (void)hipFuncSetAttribute((const void*)fused, hipFuncAttributeMaxDynamicSharedMemorySize, L_END);
  fused<<<NWG, NTHR, L_END, stream>>>((const u16*)d_in[0], (const u16*)d_in[1], (const u16*)d_in[2], (const u16*)d_in[3], (const u16*)d_in[4],
      (const u16*)d_in[6], (const u16*)d_in[7], (const u16*)d_in[8], (u16*)d_out, (unsigned char*)d_ws
      );
}
```

```cpp
#include <hip/hip_runtime.h>
#include <hip/hip_bf16.h>
#include <stdint.h>

typedef unsigned short u16;
typedef unsigned long long u64;
typedef unsigned v4u __attribute__((ext_vector_type(4)));
typedef unsigned v2u __attribute__((ext_vector_type(2)));
typedef float f32x4 __attribute__((ext_vector_type(4)));
typedef float f32x2 __attribute__((ext_vector_type(2)));
typedef short s16x4 __attribute__((ext_vector_type(4)));
typedef __bf16 bf16x2 __attribute__((ext_vector_type(2)));
typedef __bf16 bf16x8 __attribute__((ext_vector_type(8)));
#define LAS __attribute__((address_space(3)))
#define GAS __attribute__((address_space(1)))

constexpr int D = 576, DH = 128, HD = 32 * DH;
constexpr int NWG = 256, NTHR = 576;
constexpr int NU = 128, UB = 16384;
constexpr unsigned LENS = 0x11223456u;
constexpr int lsum(int n) { return n == 0 ? 0 : lsum(n - 1) + (int)((LENS >> (4 * (n - 1))) & 15u); }
constexpr u64 STARTS = (u64)lsum(1) << 8 | (u64)lsum(2) << 16 | (u64)lsum(3) << 24 | (u64)lsum(4) << 32 | (u64)lsum(5) << 40 | (u64)lsum(6) << 48 | (u64)lsum(7) << 56;
constexpr int SSUM = NU - lsum(8), NREC = 12;
constexpr int SM = 25, SW = 27;
static_assert(2 * SM + 2 * SW == SSUM, "");
constexpr int NPU = 8;
constexpr int WTHR = 48, MTHR = 56;
constexpr unsigned WS_E = 0, WS_TK = 4096, WS_S1 = WS_TK + 8 * 256, S1_STR = 640, WS_S2 = WS_S1 + 64 * NREC * S1_STR, WS_SQ = WS_S2 + 8 * D * 8,
                   WS_S3 = WS_SQ + 64 * 512, WS_END = WS_S3 + 144 * D * 16;
constexpr unsigned L_W = 0, L_WD = 73728, L_WD2 = L_WD + 32 * 1056, L_WEND = L_WD2 + 4096;
constexpr unsigned L_QPART = 0, L_XW = 4096;
constexpr unsigned L_Q = L_WEND, L_IT = L_Q + 2048;
constexpr unsigned L_QP = L_IT + 16;
constexpr unsigned L_H = L_QP + 2 * 8 * 132 * 4;
constexpr unsigned L_RED = L_H + D * 4;
constexpr unsigned L_PRIV = L_RED + 320 * 48;
constexpr unsigned L_END = L_PRIV + 9 * 576;
constexpr int AUX_NT = 2, AUX_SC1 = 16, RS3 = 0x00020000, KV_BYTES = 128 << 20;
constexpr unsigned SPIN_CAP = 1u << 17;
constexpr float L2E = 1.4426950408889634f, SCALE = 0.08838834764831845f;

#define F(u) __uint_as_float(u)
#define U(f) __float_as_uint(f)
#define BAR() asm volatile("s_waitcnt lgkmcnt(0)\n\ts_barrier" ::: "memory")
#define RAWBAR() asm volatile("s_barrier" ::: "memory")
#define LWAIT() asm volatile("s_waitcnt lgkmcnt(0)" ::: "memory")
#define SB() __builtin_amdgcn_sched_barrier(0)
#define VM0() __builtin_amdgcn_s_waitcnt(0x0F70)

__device__ __forceinline__ float bflo(unsigned u) { return F(u << 16); }
__device__ __forceinline__ float bfhi(unsigned u) { return F(u & 0xFFFF0000u); }
__device__ __forceinline__ unsigned cvtpk(float lo, float hi) {
  const bf16x2 r = {(__bf16)lo, (__bf16)hi};
  return __builtin_bit_cast(unsigned, r);
}
__device__ __forceinline__ float rbf(float f) { return bflo(cvtpk(f, f)); }
__device__ __forceinline__ float dot2(unsigned a, unsigned b, float c) {
  return __builtin_amdgcn_fdot2_f32_bf16(__builtin_bit_cast(bf16x2, a), __builtin_bit_cast(bf16x2, b), c, false);
}
template <int CTRL> __device__ __forceinline__ float dpp(float x) {
  return __builtin_bit_cast(float, __builtin_amdgcn_mov_dpp(__builtin_bit_cast(int, x), CTRL, 0xf, 0xf, true));
}
__device__ __forceinline__ float vmax(float a, float b) { float r; asm("v_max_f32 %0, %1, %2" : "=v"(r) : "v"(a), "v"(b)); return r; }
#define DPPMAX(rot, x) asm("s_nop 1\n\tv_max_f32_dpp %0, %0, %0 row_ror:" #rot " row_mask:0xf bank_mask:0xf" : "+v"(x))
__device__ __forceinline__ float row_sum(float d) { d += dpp<0x128>(d); d += dpp<0x124>(d); d += dpp<0x122>(d); d += dpp<0x121>(d); return d; }
__device__ __forceinline__ void row_sum2(float& a, float& b) {
  asm("s_nop 2\n\t"
      "v_add_f32_dpp %0, %0, %0 row_ror:8 row_mask:0xf bank_mask:0xf\n\tv_add_f32_dpp %1, %1, %1 row_ror:8 row_mask:0xf bank_mask:0xf\n\ts_nop 0\n\t"
      "v_add_f32_dpp %0, %0, %0 row_ror:4 row_mask:0xf bank_mask:0xf\n\tv_add_f32_dpp %1, %1, %1 row_ror:4 row_mask:0xf bank_mask:0xf\n\ts_nop 0\n\t"
      "v_add_f32_dpp %0, %0, %0 row_ror:2 row_mask:0xf bank_mask:0xf\n\tv_add_f32_dpp %1, %1, %1 row_ror:2 row_mask:0xf bank_mask:0xf\n\ts_nop 0\n\t"
      "v_add_f32_dpp %0, %0, %0 row_ror:1 row_mask:0xf bank_mask:0xf\n\tv_add_f32_dpp %1, %1, %1 row_ror:1 row_mask:0xf bank_mask:0xf"
      : "+v"(a), "+v"(b));
}
__device__ __forceinline__ float sw32_sum(float a, float b) {
  auto t = __builtin_amdgcn_permlane32_swap(U(a), U(b), false, false);
  return F(t[0]) + F(t[1]);
}
__device__ __forceinline__ float sw16_sum(float a, float b) {
  auto t = __builtin_amdgcn_permlane16_swap(U(a), U(b), false, false);
  return F(t[0]) + F(t[1]);
}
__device__ __forceinline__ float xrow_sum(float x) { x = sw16_sum(x, x); return sw32_sum(x, x); }
__device__ __forceinline__ float xrow_max(float x) {
  auto s = __builtin_amdgcn_permlane16_swap(U(x), U(x), false, false);
  x = vmax(F(s[0]), F(s[1]));
  auto t = __builtin_amdgcn_permlane32_swap(U(x), U(x), false, false);
  return vmax(F(t[0]), F(t[1]));
}
__device__ __forceinline__ f32x4 mfma(v4u a, v4u b, f32x4 c) {
  return __builtin_amdgcn_mfma_f32_16x16x32_bf16(__builtin_bit_cast(bf16x8, a), __builtin_bit_cast(bf16x8, b), c, 0, 0, 0);
}
__device__ __forceinline__ v4u trfrag(LAS unsigned char* L, unsigned a0, unsigned a1) {
  const s16x4 x = __builtin_amdgcn_ds_read_tr16_b64_v4i16((LAS s16x4*)(L + a0));
  const s16x4 y = __builtin_amdgcn_ds_read_tr16_b64_v4i16((LAS s16x4*)(L + a1));
  const v2u p = __builtin_bit_cast(v2u, x), q = __builtin_bit_cast(v2u, y);
  return v4u{p.x, p.y, q.x, q.y};
}
__device__ __forceinline__ int kpos(int k) { return k < 16 ? 8 * (k >> 2) + (k & 3) : 8 * ((k - 16) >> 2) + 4 + (k & 3); }
__device__ __forceinline__ void ln_consts(float s1, float s2, float& mb, float& rd) {
  const float mean = s1 * (1.f / D), var = fmaxf(fmaf(-mean, mean, s2 * (1.f / D)), 0.f);
  mb = rbf(mean);
  rd = __builtin_amdgcn_rcpf(rbf(__builtin_amdgcn_sqrtf(rbf(rbf(var) + 1e-5f))));
}

__device__ __forceinline__ void st16(__amdgpu_buffer_rsrc_t rs, v4u d, unsigned off) {
  __builtin_amdgcn_raw_buffer_store_b128(d, rs, off, 0, AUX_SC1);
  asm volatile("s_nop 1" :: "v"(d));
}
#define BADM(v) (__builtin_amdgcn_ballot_w64((v).y != T) | __builtin_amdgcn_ballot_w64((v).w != T))
#define MBEG "s_nop 4\n\t"
#define ML(i, o) "s_mov_b64 exec, %[m" #i "]\n\tbuffer_load_dwordx4 %[v" #i "], %[o" #o "], %[rs], %[s" #i "] offen sc1\n\t"
#define MEND "s_mov_b64 exec, -1\n\ts_waitcnt vmcnt(0)"
#define POLL4(V, MK, OFF, S0, STR) asm volatile(MBEG ML(0, a) ML(1, a) ML(2, a) ML(3, a) MEND \
      : [v0] "+v"(V[0]), [v1] "+v"(V[1]), [v2] "+v"(V[2]), [v3] "+v"(V[3]) \
      : [m0] "s"(MK[0]), [m1] "s"(MK[1]), [m2] "s"(MK[2]), [m3] "s"(MK[3]), \
        [s0] "s"(S0), [s1] "s"(S0 + STR), [s2] "s"(S0 + 2 * STR), [s3] "s"(S0 + 3 * STR), [oa] "v"(OFF), [rs] "s"(rsw) : "memory")
#define SEL4(d, i, e) ((U(d[i].e) & m0) | (U(d[i + 1].e) & m1) | (U(d[i + 2].e) & m2) | (U(d[i + 3].e) & m3))

__device__ __forceinline__ float kdot(const v4u k, const v4u qv) { return dot2(k.w, qv.w, dot2(k.z, qv.z, dot2(k.y, qv.y, dot2(k.x, qv.x, 0.f)))); }
__device__ __forceinline__ void rest8(float d0, float d1, const v4u v0, const v4u v1, float& m, float& l, float (&acc)[8]) {
  row_sum2(d0, d1);
  unsigned p = cvtpk(d0, d1);
  p = cvtpk(bflo(p) * SCALE, bfhi(p) * SCALE);
  const float s0 = bflo(p) * L2E, s1 = bfhi(p) * L2E;
  const float mn = fmaxf(fmaxf(m, s0), s1);
  const float al = __builtin_amdgcn_exp2f(m - mn), p0 = __builtin_amdgcn_exp2f(s0 - mn), p1 = __builtin_amdgcn_exp2f(s1 - mn);
  m = mn; l = fmaf(l, al, p0 + p1);
#pragma unroll
  for (int j = 0; j < 4; ++j) {
    acc[2 * j] = fmaf(p1, bflo(v1[j]), fmaf(p0, bflo(v0[j]), acc[2 * j] * al));
    acc[2 * j + 1] = fmaf(p1, bfhi(v1[j]), fmaf(p0, bfhi(v0[j]), acc[2 * j + 1] * al));
  }
}
__device__ __forceinline__ void merge_wave(LAS unsigned char* L, unsigned rb, float m, float l, const float (&acc)[8], int w, int lane) {
  const float M = xrow_max(m), f = __builtin_amdgcn_exp2f(m - M);
  const float Lw = xrow_sum(l * f);
  const float z0 = sw32_sum(acc[0] * f, acc[4] * f), z1 = sw32_sum(acc[1] * f, acc[5] * f), z2 = sw32_sum(acc[2] * f, acc[6] * f), z3 = sw32_sum(acc[3] * f, acc[7] * f);
  *(LAS f32x2*)(L + rb + (w * 132 + 4 + 8 * (lane & 15) + 2 * (lane >> 4)) * 4) = f32x2{sw16_sum(z0, z2), sw16_sum(z1, z3)};
  if (lane == 0) *(LAS f32x2*)(L + rb + (w * 132) * 4) = f32x2{M, Lw};
}
__device__ __forceinline__ void publish(LAS unsigned char* L, unsigned rb, __amdgpu_buffer_rsrc_t rws, unsigned T, unsigned off, int lane) {
  const f32x2 ml = *(LAS f32x2*)(L + rb + ((lane & 7) * 132) * 4);
  f32x2 o[8];
#pragma unroll
  for (int i = 0; i < 8; ++i) o[i] = *(LAS f32x2*)(L + rb + (i * 132 + 4 + 2 * lane) * 4);
  float MM = ml.x;
  DPPMAX(4, MM); DPPMAX(2, MM); DPPMAX(1, MM);
  const float wt = __builtin_amdgcn_exp2f(ml.x - MM);
  float LL = wt * ml.y; LL += dpp<0x124>(LL); LL += dpp<0x122>(LL); LL += dpp<0x121>(LL);
  float o0 = dpp<0x150>(wt) * o[0].x, o1 = dpp<0x150>(wt) * o[0].y;
#define ADDW(i) o0 = fmaf(dpp<0x150 + i>(wt), o[i].x, o0); o1 = fmaf(dpp<0x150 + i>(wt), o[i].y, o1);
  ADDW(1) ADDW(2) ADDW(3) ADDW(4) ADDW(5) ADDW(6) ADDW(7)
  const float rl = __builtin_amdgcn_rcpf(LL);
  const unsigned pk = cvtpk(o0 * rl, o1 * rl), pn = (unsigned)__builtin_amdgcn_mov_dpp((int)pk, 0xB1, 0xf, 0xf, true), ls = U(MM + __builtin_amdgcn_logf(LL));
  st16(rws, v4u{(lane & 1) ? ls : pk, T, (lane & 1) ? ls : pn, T}, off);
}

__global__ void __launch_bounds__(NTHR, 3) fused(const u16* __restrict__ x, const u16* __restrict__ K, const u16* __restrict__ V, const u16* __restrict__ wq,
    const u16* __restrict__ wo, const u16* __restrict__ wg, const u16* __restrict__ wu, const u16* __restrict__ wd, u16* __restrict__ out, unsigned char* ws) {
  extern __shared__ __attribute__((aligned(16))) unsigned char smem[];
  LAS unsigned char* L = (LAS unsigned char*)smem;
  const f32x4 Z4 = {0.f, 0.f, 0.f, 0.f};
  const int tid = threadIdx.x, lane = tid & 63, wv = __builtin_amdgcn_readfirstlane(tid >> 6), r = lane >> 4, c = lane & 15, vo = lane * 16;
  const int g = blockIdx.x, x8 = g & 7, j = g >> 3, jg = j >> 2, h = x8 * 4 + (j >> 3), b = jg & 1, s = j & 3, G = b * 32 + h;
  const int pool = (jg + 2 * x8) & 7;
  const int grp = (jg + x8) & 7;
  const bool is_mlp = s < 2 || (s == 2 && grp < 2), is_wo = !is_mlp && (s == 2 || grp >= 5);
  const int cg = s == 2 ? grp - 2 : grp + 1, mi = x8 * 18 + (s < 2 ? 2 * grp + s : 16 + grp);
  const int t2 = grp < 2 ? SM : SW;
  const int slo = s < 3 ? s * SM : 2 * SM + t2, SS = (s < 2 ? SM : s == 2 ? t2 : SSUM - 2 * SM - t2) - NPU;
  const __amdgpu_buffer_rsrc_t rws = __builtin_amdgcn_make_buffer_rsrc((void*)ws, 0, (int)WS_END, RS3);
  const v4u rsw = {(unsigned)(size_t)ws, (unsigned)((size_t)ws >> 32) & 0xFFFFu, WS_END, (unsigned)RS3};
  unsigned T;
  if (wv == 0) {
    T = __builtin_amdgcn_readfirstlane(__builtin_amdgcn_raw_buffer_load_b32(rws, 0, WS_E, AUX_SC1)) + 1u;
    *(volatile LAS int*)(L + L_IT) = 0;
    *(LAS unsigned*)(L + L_IT + 4) = T;
    BAR();
    {
      RAWBAR(); RAWBAR(); RAWBAR();
      if (x8 == 0 && s == 0 && lane == 0) __hip_atomic_store((GAS u64*)(ws + WS_TK + pool * 256), (u64)T << 32, __ATOMIC_RELAXED, __HIP_MEMORY_SCOPE_AGENT);
      v4u pq[4];
      u64 mq[4];
      const unsigned oq = (pool * 8 + (lane >> 5)) * 512 + (lane & 31) * 16;
#pragma unroll
      for (int i = 0; i < 4; ++i) pq[i] = __builtin_amdgcn_raw_buffer_load_b128(rws, oq, WS_SQ + i * 1024, AUX_SC1);
      SB();
      RAWBAR();
#pragma nounroll
      for (unsigned spin = 0; spin < SPIN_CAP; ++spin) {
        u64 any = 0;
#pragma unroll
        for (int i = 0; i < 4; ++i) { mq[i] = BADM(pq[i]); any |= mq[i]; }
        if (any == 0) break;
        POLL4(pq, mq, oq, WS_SQ, 1024u);
      }
#pragma unroll
      for (int i = 0; i < 4; ++i) *(LAS v2u*)(L + L_Q + i * 512 + lane * 8) = v2u{pq[i].x, pq[i].z};
    }
    __builtin_amdgcn_s_setprio(3);
    const int sc = (lane & 7) ^ (r << 1);
    const unsigned char* src = is_wo ? (const unsigned char*)wo + ((size_t)(512 * x8 + (lane >> 3)) * D + 64 * cg) * 2 + 16 * sc
                                     : (const unsigned char*)(sc < 4 ? wg : wu) + (size_t)(lane >> 3) * 9216 + 64 * mi + 16 * (sc & 3);
    const int ph = is_wo ? 32 : is_mlp ? 36 : 0, k0 = 4 + (g * 37 >> 3) % (SS - 7), adv = is_wo ? 8 * 1152 : 8 * 9216;
    const unsigned char* sd = (const unsigned char*)wd + (unsigned)(32 * mi * 1152 + lane * 16);
    const unsigned char* sd2 = (const unsigned char*)wd + (unsigned)((32 * mi + (lane >> 3)) * 1152 + 1024 + 16 * sc);
    if (is_mlp) {
#pragma unroll
      for (int i = 0; i < 9; ++i) *(LAS unsigned*)(L + L_H + (64 * i + lane) * 4) = (unsigned)x[64 * i + lane] | ((unsigned)x[D + 64 * i + lane] << 16);
    }
    u64 tk = 0;
    const unsigned tko = WS_TK + pool * 256;
#define DRAW(inc) do { const u64 inc_ = (inc); asm volatile("s_mov_b64 exec, 1\n\tglobal_atomic_add_x2 %0, %1, %2, %3 sc0\n\ts_mov_b64 exec, -1" : "+v"(tk) : "v"(tko), "v"(inc_), "s"(ws) : "memory"); } while (0)
#define DRAWN() asm volatile("s_waitcnt vmcnt(0)" : "+v"(tk) :: "memory")
    int p = 0, seen = 0, due = SS - 1, cnt = 0;
#pragma nounroll
    for (int k = 4;; ++k) {
      if (k == due) {
        if (is_wo && cnt == 0) {
          DRAWN();
          seen = (unsigned)__builtin_amdgcn_readfirstlane((unsigned)(tk >> 32)) == T ? (int)__builtin_amdgcn_readfirstlane((unsigned)tk) : 64;
        }
        int cur = -1;
        if (seen < (is_wo ? WTHR : is_mlp ? MTHR : 64)) {
#pragma nounroll
          for (unsigned spin = 0; spin < SPIN_CAP; ++spin) {
            DRAW(1);
            DRAWN();
            if ((unsigned)__builtin_amdgcn_readfirstlane((unsigned)(tk >> 32)) == T) break;
          }
          const unsigned it = __builtin_amdgcn_readfirstlane((unsigned)tk);
          cur = ((unsigned)__builtin_amdgcn_readfirstlane((unsigned)(tk >> 32)) == T && it < 64) ? (int)it : -1;
        }
        ++cnt; seen = cur;
        *(volatile LAS int*)(L + L_IT) = cnt * 256 + cur + 1;
        if (cur >= 0) due += (LENS >> (4 * (cur >> 3))) & 15u;
      }
      if (is_wo && k == SS - 2) DRAW(0);
      const int pe = (k == k0 || k == k0 + 1) ? p + ph : p;
#pragma nounroll
      for (; p < pe; ++p) { __builtin_amdgcn_global_load_lds((const GAS void*)src, (LAS void*)(L + L_W + p * 1024), 16, 0, 0); src += adv; }
      if (k == SS - 1) VM0();
      BAR();
      if (k >= due) break;
    }
    if (!is_mlp) return;
    RAWBAR();
#pragma nounroll
    for (int q = 0; q < 32; ++q) { __builtin_amdgcn_global_load_lds((const GAS void*)sd, (LAS void*)(L + L_WD + q * 1056), 16, 0, 0); sd += 1152; }
#pragma unroll
    for (int e = 0; e < 4; ++e) __builtin_amdgcn_global_load_lds((const GAS void*)(sd2 + e * 8 * 1152), (LAS void*)(L + L_WD2 + e * 1024), 16, 0, 0);
  } else {
    const int w = wv - 1;
#define UOFF(G_, u_) (((G_) * NU + (u_)) * UB + w * 2048)
#define PIN() asm volatile("" : "+v"(m), "+v"(l), "+v"(acc[0]), "+v"(acc[1]), "+v"(acc[2]), "+v"(acc[3]), "+v"(acc[4]), "+v"(acc[5]), "+v"(acc[6]), "+v"(acc[7]) :: "memory")
    float m = -INFINITY, l = 0.f, acc[8];
#pragma unroll
    for (int i = 0; i < 8; ++i) acc[i] = 0.f;
    {
      v4u A[4], P[5][4];
      const __amdgpu_buffer_rsrc_t rk = __builtin_amdgcn_make_buffer_rsrc((void*)K, 0, KV_BYTES, RS3);
      const __amdgpu_buffer_rsrc_t rv = __builtin_amdgcn_make_buffer_rsrc((void*)V, 0, KV_BYTES, RS3);
#define LD1(X, e, u_) X[e] = __builtin_amdgcn_raw_buffer_load_b128((e) < 2 ? rk : rv, vo + ((e) & 1) * 1024, UOFF(G, slo + (u_)), AUX_NT)
#define LDP(X, u_) do { LD1(X, 0, u_); LD1(X, 1, u_); LD1(X, 2, u_); LD1(X, 3, u_); SB(); } while (0)
      v4u qv;
      {
        const __amdgpu_buffer_rsrc_t rq = __builtin_amdgcn_make_buffer_rsrc((void*)wq, 0, D * HD * 2, RS3);
        const __amdgpu_buffer_rsrc_t rx = __builtin_amdgcn_make_buffer_rsrc((void*)(x + b * D), 0, D * 2, RS3);
        const v4u xa = __builtin_amdgcn_raw_buffer_load_b128(rx, vo, 0, 0), xb = __builtin_amdgcn_raw_buffer_load_b128(rx, vo + 1024, 0, 0);
        const int qo = (18 * r * HD + 8 * c) * 2, qs = (72 * w * HD + h * DH) * 2;
        v4u wr[18];
#define LDW(i) wr[i] = __builtin_amdgcn_raw_buffer_load_b128(rq, qo, qs + (i) * HD * 2, 0)
        __builtin_amdgcn_s_setprio(3);
#pragma unroll
        for (int i = 0; i < 4; ++i) { LDW(i); LD1(A, i, 0); SB(); }
#pragma unroll
        for (int i = 0; i < 4; ++i) { LDW(4 + i); LD1(P[0], i, 1); SB(); }
        const unsigned xl = L_XW + w * 2048;
        *(LAS v4u*)(L + xl + vo) = xa; *(LAS v4u*)(L + xl + 1024 + vo) = xb;
        unsigned xr[9];
#pragma unroll
        for (int p = 0; p < 9; ++p) xr[p] = *(LAS unsigned*)(L + xl + (72 * w + 18 * r) * 2 + 4 * p);
        float s1 = 0.f, s2 = 0.f;
#pragma unroll
        for (int i = 0; i < 4; ++i) { s1 = dot2(xb[i], 0x3F803F80u, dot2(xa[i], 0x3F803F80u, s1)); s2 = dot2(xb[i], xb[i], dot2(xa[i], xa[i], s2)); }
        float mb, rd;
        ln_consts(xrow_sum(row_sum(s1)), xrow_sum(row_sum(s2)), mb, rd);
        unsigned lp[9];
#pragma unroll
        for (int p = 0; p < 9; ++p) { const unsigned t = cvtpk(bflo(xr[p]) - mb, bfhi(xr[p]) - mb); lp[p] = cvtpk(bflo(t) * rd, bfhi(t) * rd); }
        float qa[8];
#pragma unroll
        for (int i = 0; i < 8; ++i) qa[i] = 0.f;
        SB();
#pragma unroll
        for (int p = 0; p < 9; ++p) {
#pragma unroll
          for (int d = 0; d < 4; ++d) {
            qa[2 * d] = dot2(__builtin_amdgcn_perm(wr[2 * p + 1][d], wr[2 * p][d], 0x05040100u), lp[p], qa[2 * d]);
            qa[2 * d + 1] = dot2(__builtin_amdgcn_perm(wr[2 * p + 1][d], wr[2 * p][d], 0x07060302u), lp[p], qa[2 * d + 1]);
          }
          SB();
          if (p == 1) {
#pragma unroll
            for (int i = 0; i < 10; ++i) { LDW(8 + i); if (i < 8) LD1(P[1 + (i >> 2)], i & 3, 2 + (i >> 2)); SB(); }
            __builtin_amdgcn_s_setprio(0);
          }
        }
        LDP(P[3], 4); LDP(P[4], 5);
        const float z0 = sw32_sum(qa[0], qa[4]), z1 = sw32_sum(qa[1], qa[5]), z2 = sw32_sum(qa[2], qa[6]), z3 = sw32_sum(qa[3], qa[7]);
        *(LAS f32x2*)(L + L_QPART + (w * 128 + 8 * c + 2 * r) * 4) = f32x2{sw16_sum(z0, z2), sw16_sum(z1, z3)};
        BAR();
        const f32x4 q0 = *(LAS f32x4*)(L + L_QPART + (r * 256 + 8 * c) * 4) + *(LAS f32x4*)(L + L_QPART + (r * 256 + 128 + 8 * c) * 4);
        const f32x4 q1 = *(LAS f32x4*)(L + L_QPART + (r * 256 + 8 * c + 4) * 4) + *(LAS f32x4*)(L + L_QPART + (r * 256 + 128 + 8 * c + 4) * 4);
        const float y0 = sw32_sum(q0.x, q1.x), y1 = sw32_sum(q0.y, q1.y), y2 = sw32_sum(q0.z, q1.z), y3 = sw32_sum(q0.w, q1.w);
        const unsigned pk = cvtpk(sw16_sum(y0, y2), sw16_sum(y1, y3));
        auto t16 = __builtin_amdgcn_permlane16_swap(pk, pk, false, false);
        auto te = __builtin_amdgcn_permlane32_swap(t16[0], t16[0], false, false), to = __builtin_amdgcn_permlane32_swap(t16[1], t16[1], false, false);
        qv = v4u{te[0], to[0], te[1], to[1]};
        T = __builtin_amdgcn_readfirstlane(*(LAS unsigned*)(L + L_IT + 4));
      }
#define USE(X) do { rest8(kdot(X[0], qv), kdot(X[1], qv), X[2], X[3], m, l, acc); PIN(); SB(); } while (0)
      USE(A);
      LDP(A, 6);
      if (s == 0 && w == 0 && r < 2) st16(rws, v4u{r ? qv.z : qv.x, T, r ? qv.w : qv.y, T}, WS_SQ + ((pool * 8 + x8) * 32 + 2 * c + r) * 16);
      USE(P[0]); USE(P[1]);
      LDP(P[0], 7);
      USE(P[2]); USE(P[3]); USE(P[4]);
      USE(A);
      LDP(A, 8);
      USE(P[0]);
      int rec = (h * 2 + b) * NREC + s, rem = SS, cnt = 0, Gc = G, uc = slo + NPU;
      bool live = true;
#pragma nounroll
      do {
        const bool fin = rem == 1;
        const unsigned rb = L_QP + (cnt & 1) * 4224, ro = WS_S1 + rec * S1_STR + ((lane & 1) ? 512 : lane * 8);
        float d0 = kdot(A[0], qv), d1 = kdot(A[1], qv);
        asm volatile("" : "+v"(d0), "+v"(d1) :: "memory");
        SB();
        --rem; ++uc;
        {
          const __amdgpu_buffer_rsrc_t rk_ = __builtin_amdgcn_make_buffer_rsrc((void*)K, 0, fin ? 0 : KV_BYTES, RS3);
          const int so = fin ? 0 : UOFF(Gc, uc);
          SB();
          A[0] = __builtin_amdgcn_raw_buffer_load_b128(rk_, vo, so, AUX_NT); A[1] = __builtin_amdgcn_raw_buffer_load_b128(rk_, vo + 1024, so, AUX_NT);
          SB();
        }
        rest8(d0, d1, A[2], A[3], m, l, acc);
        PIN(); SB();
        if (fin) {
          ++cnt;
          int wd_;
#pragma nounroll
          for (unsigned spin = 0; spin < (SPIN_CAP << 4); ++spin) {
            wd_ = __builtin_amdgcn_readfirstlane(*(volatile LAS int*)(L + L_IT));
            if ((wd_ >> 8) == cnt) break;
          }
          const int it = (wd_ >> 8) == cnt ? (wd_ & 255) - 1 : -1;
          live = it >= 0;
          const int itx = it & 7, h2 = itx * 4 + (((pool - 2 * itx) & 7) >> 1), pc = (it >> 3) & 7;
          Gc = b * 32 + h2; uc = SSUM + (int)((STARTS >> (8 * pc)) & 255u); rem = (LENS >> (4 * pc)) & 15u;
          rec = (h2 * 2 + b) * NREC + 4 + pc;
          qv = *(LAS v4u*)(L + L_Q + itx * 256 + c * 16);
          const v4u rk4 = {(unsigned)(size_t)K, (unsigned)((size_t)K >> 32) & 0xFFFFu, live ? (unsigned)KV_BYTES : 0u, (unsigned)RS3};
          const int so = live ? UOFF(Gc, uc) : 0;
          asm volatile("s_nop 4\n\tbuffer_load_dwordx4 %0, %2, %3, %4 offen nt\n\tbuffer_load_dwordx4 %1, %2, %3, %4 offen offset:1024 nt"
                       : "+v"(A[0]), "+v"(A[1]) : "v"(vo), "s"(rk4), "s"(so) : "memory");
          SB();
        }
        {
          const __amdgpu_buffer_rsrc_t rv_ = __builtin_amdgcn_make_buffer_rsrc((void*)V, 0, live ? KV_BYTES : 0, RS3);
          const int so = live ? UOFF(Gc, uc) : 0;
          SB();
          A[2] = __builtin_amdgcn_raw_buffer_load_b128(rv_, vo, so, AUX_NT); A[3] = __builtin_amdgcn_raw_buffer_load_b128(rv_, vo + 1024, so, AUX_NT);
          SB();
        }
        if (fin) {
          merge_wave(L, rb, m, l, acc, w, lane);
          m = -INFINITY; l = 0.f;
#pragma unroll
          for (int i = 0; i < 8; ++i) acc[i] = 0.f;
        }
        BAR();
        if (fin && w == 0) publish(L, rb, rws, T, ro, lane);
      } while (live);
      asm volatile("s_waitcnt vmcnt(2)" : "+v"(A[0]), "+v"(A[1]) :: "memory");
    }
    if (!is_mlp) {
      if (is_wo) {
        const int q4 = c >> 2, p4 = c & 3, fr = ((2 * r + (q4 >> 1)) & 3) << 1;
        v4u bf[2][4];
#pragma unroll
        for (int ks = 0; ks < 2; ++ks)
#pragma unroll
          for (int t = 0; t < 4; ++t) {
            const unsigned a = L_W + (64 * w + 32 * ks + 4 * r + q4) * 128 + (((2 * t + (p4 >> 1)) ^ fr) * 16) + 8 * (p4 & 1);
            bf[ks][t] = trfrag(L, a, a + 16 * 128);
          }
        const int pb = lane >> 5, k2 = 4 * c + 2 * (r & 1);
        const unsigned ob = (((4 * x8 + (w >> 1)) * 2 + pb) * NREC + 6 * (r & 1)) * S1_STR;
        unsigned o1 = ob + (16 * (w & 1) + c) * 16, o2 = ob + ((c & 7) < 6 ? (c & 7) : 5) * S1_STR + 512, m6 = (c & 7) < 6 ? ~0u : 0u;
        unsigned aw = L_PRIV + w * 576 + pb * 128 + (k2 & 32) * 2 + kpos(k2 & 31) * 2, ar = L_PRIV + w * 576 + (c & 1) * 128 + r * 16, ad = L_RED + (w * 64 + lane) * 8;
        unsigned m0 = r == 0 ? ~0u : 0u, m1 = r == 1 ? ~0u : 0u, m2 = r == 2 ? ~0u : 0u, m3 = r == 3 ? ~0u : 0u;
        asm volatile("" : "+v"(o1), "+v"(o2), "+v"(m6), "+v"(aw), "+v"(ar), "+v"(ad), "+v"(m0), "+v"(m1), "+v"(m2), "+v"(m3));
#pragma unroll
        for (int t = 0; t < 4; ++t) asm volatile("" : "+v"(bf[0][t]), "+v"(bf[1][t]));
        v4u ov[7];
        u64 mk[7];
#pragma unroll
        for (int i = 0; i < 7; ++i) mk[i] = ~0ull;
#pragma nounroll
        for (unsigned spin = 0; spin < SPIN_CAP; ++spin) {
          asm volatile(MBEG ML(6, b) ML(0, a) ML(1, a) ML(2, a) ML(3, a) ML(4, a) ML(5, a) MEND
              : [v0] "+v"(ov[0]), [v1] "+v"(ov[1]), [v2] "+v"(ov[2]), [v3] "+v"(ov[3]), [v4] "+v"(ov[4]), [v5] "+v"(ov[5]), [v6] "+v"(ov[6])
              : [m0] "s"(mk[0]), [m1] "s"(mk[1]), [m2] "s"(mk[2]), [m3] "s"(mk[3]), [m4] "s"(mk[4]), [m5] "s"(mk[5]), [m6] "s"(mk[6]),
                [s0] "s"(WS_S1), [s1] "s"(WS_S1 + S1_STR), [s2] "s"(WS_S1 + 2 * S1_STR), [s3] "s"(WS_S1 + 3 * S1_STR), [s4] "s"(WS_S1 + 4 * S1_STR), [s5] "s"(WS_S1 + 5 * S1_STR),
                [s6] "s"(WS_S1), [oa] "v"(o1), [ob] "v"(o2), [rs] "s"(rsw) : "memory");
          u64 any = 0;
#pragma unroll
          for (int i = 0; i < 7; ++i) { mk[i] = BADM(ov[i]); any |= mk[i]; }
          if (any == 0) break;
        }
        float Mx = F(ov[6].x);
        DPPMAX(4, Mx); DPPMAX(2, Mx); DPPMAX(1, Mx);
        { auto t = __builtin_amdgcn_permlane16_swap(U(Mx), U(Mx), false, false); Mx = vmax(F(t[0]), F(t[1])); }
        const float wt = __builtin_amdgcn_exp2f(F(ov[6].x) - Mx);
        float LL = F(U(wt) & m6);
        LL += dpp<0x124>(LL); LL += dpp<0x122>(LL); LL += dpp<0x121>(LL);
        LL = sw16_sum(LL, LL);
        float e0, e1, e2, e3;
        { const float wi = dpp<0x150>(wt); e0 = wi * bflo(ov[0].x); e1 = wi * bfhi(ov[0].x); e2 = wi * bflo(ov[0].z); e3 = wi * bfhi(ov[0].z); }
#define ADDREC(i) { const float wi = dpp<0x150 + i>(wt); e0 = fmaf(wi, bflo(ov[i].x), e0); e1 = fmaf(wi, bfhi(ov[i].x), e1); e2 = fmaf(wi, bflo(ov[i].z), e2); e3 = fmaf(wi, bfhi(ov[i].z), e3); }
        ADDREC(1) ADDREC(2) ADDREC(3) ADDREC(4) ADDREC(5)
        const float rl = __builtin_amdgcn_rcpf(LL);
        *(LAS unsigned*)(L + aw) = cvtpk(sw16_sum(e0, e2) * rl, sw16_sum(e1, e3) * rl);
        LWAIT();
        const v4u a0 = *(LAS v4u*)(L + ar), a1 = *(LAS v4u*)(L + ar + 64);
        f32x4 d[4];
#pragma unroll
        for (int t = 0; t < 4; ++t) d[t] = mfma(a0, bf[0][t], Z4);
#pragma unroll
        for (int t = 0; t < 4; ++t) d[t] = mfma(a1, bf[1][t], d[t]);
        *(LAS v2u*)(L + ad) = v2u{SEL4(d, 0, x), SEL4(d, 0, y)};
        BAR();
        if (w == 0) {
          f32x2 y[8];
#pragma unroll
          for (int i = 0; i < 8; ++i) y[i] = *(LAS f32x2*)(L + L_RED + (i * 64 + lane) * 8);
          const f32x2 yy = ((y[0] + y[1]) + (y[2] + y[3])) + ((y[4] + y[5]) + (y[6] + y[7]));
          const unsigned pk = cvtpk(yy.x, yy.y), pn = (unsigned)__builtin_amdgcn_mov_dpp((int)pk, 0xB1, 0xf, 0xf, true);
          if (!(lane & 1)) st16(rws, v4u{pk, T, pn, T}, lane * 8 + WS_S2 + (x8 * D + 64 * cg) * 8);
        }
      }
      return;
    }
    RAWBAR();
  }
  const int w = wv;
  const int q4 = c >> 2, p4 = c & 3, pb = lane >> 5, cc = lane & 31, fr = ((2 * r + (q4 >> 1)) & 3) << 1;
  const unsigned pv = L_PRIV + w * 576;
  *(LAS u16*)(L + pv + 288 + lane * 2) = (u16)0x3F80;
  const int kh = 2 * cc + pb;
  unsigned o2 = pb * D * 8 + (32 * w + cc) * 16;
  v4u yv[4];
  u64 mk[9];
#pragma unroll
  for (int i = 0; i < 9; ++i) mk[i] = ~0ull;
  const unsigned ig = ((cc >> 4) * 64 + (cc & 15)) * 48;
  unsigned i1 = L_RED + pb * 768 + ig, i3 = L_RED + 2 * 768 + ig + pb * 6144, i4 = L_RED + (256 + pb * 16 + ((cc & 1) ? 4 * pb : 8)) * 48;
  unsigned aA = pv + (c < 4 ? 0 : c < 8 ? 144 : 288) + r * 16, ah = pv + (kh >> 5) * 64 + kpos(kh & 31) * 2, aa = pv + 432 + pb * 64 + kpos(cc) * 2,
           aH = L_H + (64 * w + kh) * 4, aR = L_RED + lane * 48 + w * 4, aB = pv + 432 + (c & 1) * 64 + r * 16;
  unsigned m0 = r == 0 ? ~0u : 0u, m1 = r == 1 ? ~0u : 0u, m2 = r == 2 ? ~0u : 0u, m3 = r == 3 ? ~0u : 0u;
  unsigned ag = L_W + (64 * w + 4 * r + q4) * 128 + 8 * (p4 & 1), ad0 = L_WD + (4 * r + q4) * 1056 + 288 * (w & 3) + 8 * p4, ae0 = L_WD2 + (4 * r + q4) * 128 + 8 * (p4 & 1);
  unsigned g0 = ag + (((p4 >> 1) ^ fr) * 16), g1 = ag + (((2 + (p4 >> 1)) ^ fr) * 16), g2 = ag + (((4 + (p4 >> 1)) ^ fr) * 16), g3 = ag + (((6 + (p4 >> 1)) ^ fr) * 16);
  unsigned s3a = WS_S3 + (mi * D + 144 * w) * 16 + vo, s3b = WS_S3 + (mi * D + 144 * w + 128 + c) * 16;
  asm volatile("" : "+v"(i1), "+v"(i3), "+v"(i4), "+v"(aA), "+v"(ah), "+v"(aa), "+v"(aH), "+v"(aR), "+v"(aB), "+v"(m0), "+v"(m1), "+v"(m2), "+v"(m3));
  asm volatile("" : "+v"(g0), "+v"(g1), "+v"(g2), "+v"(g3), "+v"(ad0), "+v"(s3a), "+v"(s3b));
  unsigned xw = *(LAS unsigned*)(L + aH);
  asm volatile("" : "+v"(xw));
  VM0();
  RAWBAR();
  v4u gf[2][4], df[9];
#pragma unroll
  for (int ks = 0; ks < 2; ++ks) {
    gf[ks][0] = trfrag(L, g0 + ks * 4096, g0 + ks * 4096 + 2048); gf[ks][1] = trfrag(L, g1 + ks * 4096, g1 + ks * 4096 + 2048);
    gf[ks][2] = trfrag(L, g2 + ks * 4096, g2 + ks * 4096 + 2048); gf[ks][3] = trfrag(L, g3 + ks * 4096, g3 + ks * 4096 + 2048);
  }
  if (w < 4) {
#pragma unroll
    for (int i = 0; i < 9; ++i) {
      const bool hi = i >= 5 && w == 3;
      const unsigned a = hi ? ae0 + ((2 * (i - 5) + (p4 >> 1)) ^ fr) * 16 : ad0 + i * 32;
      df[i] = trfrag(L, a, a + (hi ? 16 * 128 : 16 * 1056));
    }
  }
#pragma unroll
  for (int t = 0; t < 4; ++t) asm volatile("" : "+v"(gf[0][t]), "+v"(gf[1][t]));
#pragma unroll
  for (int t = 0; t < 9; ++t) asm volatile("" : "+v"(df[t]));
#pragma nounroll
  for (unsigned spin = 0; spin < SPIN_CAP; ++spin) {
    POLL4(yv, mk, o2, WS_S2, 2 * D * 8);
    int miss = 0;
#pragma unroll
    for (int i = 0; i < 4; ++i) { mk[i] = BADM(yv[i]); miss += mk[i] != 0; }
    if (miss == 0) break;
    if (miss >= 3) __builtin_amdgcn_s_sleep(6); else if (miss >= 2) __builtin_amdgcn_s_sleep(2);
  }
  {
    const float y0 = sw32_sum((bflo(yv[0].x) + bflo(yv[1].x)) + (bflo(yv[2].x) + bflo(yv[3].x)), (bflo(yv[0].z) + bflo(yv[1].z)) + (bflo(yv[2].z) + bflo(yv[3].z)));
    const float y1 = sw32_sum((bfhi(yv[0].x) + bfhi(yv[1].x)) + (bfhi(yv[2].x) + bfhi(yv[3].x)), (bfhi(yv[0].z) + bfhi(yv[1].z)) + (bfhi(yv[2].z) + bfhi(yv[3].z)));
    const unsigned yb = cvtpk(y0, y1), hb = cvtpk(bflo(xw) + bflo(yb), bfhi(xw) + bfhi(yb));
    *(LAS u16*)(L + ah) = (u16)hb;
    *(LAS u16*)(L + ah + 144) = (u16)(hb >> 16);
    *(LAS unsigned*)(L + aH) = hb;
    LWAIT();
    const v4u a0 = *(LAS v4u*)(L + aA), a1 = *(LAS v4u*)(L + aA + 64);
    f32x4 d[5];
#pragma unroll
    for (int t = 0; t < 4; ++t) d[t] = mfma(a0, gf[0][t], Z4);
    d[4] = mfma(a0, a0, Z4);
#pragma unroll
    for (int t = 0; t < 4; ++t) d[t] = mfma(a1, gf[1][t], d[t]);
    d[4] = mfma(a1, a1, d[4]);
#pragma unroll
    for (int t = 0; t < 5; ++t) *(LAS float*)(L + aR + t * 3072) = d[t].x;
  }
  BAR();
  if (w < 4) {
#define SUM9(a) ({ const f32x4 t_ = *(LAS f32x4*)(L + (a)) + *(LAS f32x4*)(L + (a) + 16); ((t_.x + t_.y) + (t_.z + t_.w)) + *(LAS float*)(L + (a) + 32); })
    const float St = SUM9(i4), Gs = SUM9(i1), Us = SUM9(i1 + 6144), Cs = SUM9(i3);
    auto cs = __builtin_amdgcn_permlane32_swap(U(Cs), U(Cs), false, false);
    float mb, rd;
    ln_consts(dpp<0xA0>(St), dpp<0xF5>(St), mb, rd);
    const unsigned gu = cvtpk(fmaf(-mb, F(cs[0]), Gs) * rd, fmaf(-mb, F(cs[1]), Us) * rd);
    const float gg = bflo(gu);
    const float kk = gg * fmaf(gg * gg, -2.f * L2E * 0.7978845608028654f * 0.044715f, -2.f * L2E * 0.7978845608028654f);
    const float ge = gg * __builtin_amdgcn_rcpf(1.f + __builtin_amdgcn_exp2f(kk));
    const float act = rbf(ge) * bfhi(gu);
    *(LAS u16*)(L + aa) = (u16)cvtpk(act, act);
    LWAIT();
    const v4u a = *(LAS v4u*)(L + aB);
    f32x4 d[9];
#pragma unroll
    for (int i = 0; i < 9; ++i) d[i] = mfma(a, df[i], Z4);
    st16(rws, v4u{SEL4(d, 0, x), T, SEL4(d, 0, y), T}, s3a);
    st16(rws, v4u{SEL4(d, 4, x), T, SEL4(d, 4, y), T}, s3a + 1024);
    st16(rws, v4u{U(d[8].x), T, U(d[8].y), T}, s3b);
  }
  if (w == 4) {
    const int n = 4 * mi + (lane & 3);
    const unsigned hres = *(LAS unsigned*)(L + L_H + n * 4);
    float hr = pb ? bfhi(hres) : bflo(hres);
    unsigned o3 = ((lane >> 2) * D + n) * 16, oo = (pb * D + n) * 2;
    asm volatile("" : "+v"(hr), "+v"(o3), "+v"(oo));
    v4u zv[9];
#pragma unroll
    for (int i = 0; i < 9; ++i) mk[i] = ~0ull;
    constexpr unsigned S3S = 16 * D * 16;
    __builtin_amdgcn_s_sleep(27);
#pragma nounroll
    for (unsigned spin = 0; spin < SPIN_CAP; ++spin) {
      asm volatile(MBEG ML(0, a) ML(1, a) ML(2, a) ML(3, a) ML(4, a) ML(5, a) ML(6, a) ML(7, a) ML(8, a) MEND
          : [v0] "+v"(zv[0]), [v1] "+v"(zv[1]), [v2] "+v"(zv[2]), [v3] "+v"(zv[3]), [v4] "+v"(zv[4]), [v5] "+v"(zv[5]), [v6] "+v"(zv[6]), [v7] "+v"(zv[7]), [v8] "+v"(zv[8])
          : [m0] "s"(mk[0]), [m1] "s"(mk[1]), [m2] "s"(mk[2]), [m3] "s"(mk[3]), [m4] "s"(mk[4]), [m5] "s"(mk[5]), [m6] "s"(mk[6]), [m7] "s"(mk[7]), [m8] "s"(mk[8]),
            [s0] "s"(WS_S3), [s1] "s"(WS_S3 + S3S), [s2] "s"(WS_S3 + 2 * S3S), [s3] "s"(WS_S3 + 3 * S3S), [s4] "s"(WS_S3 + 4 * S3S), [s5] "s"(WS_S3 + 5 * S3S),
            [s6] "s"(WS_S3 + 6 * S3S), [s7] "s"(WS_S3 + 7 * S3S), [s8] "s"(WS_S3 + 8 * S3S),
            [oa] "v"(o3), [rs] "s"(rsw) : "memory");
      u64 any = 0;
#pragma unroll
      for (int i = 0; i < 9; ++i) { mk[i] = BADM(zv[i]); any |= mk[i]; }
      if (any == 0) break;
      __builtin_amdgcn_s_sleep(4);
    }
#define SUM8(V, e) (((F(V[0].e) + F(V[1].e)) + (F(V[2].e) + F(V[3].e))) + ((F(V[4].e) + F(V[5].e)) + (F(V[6].e) + F(V[7].e))))
    float zs = sw32_sum(SUM8(zv, x) + F(zv[8].x), SUM8(zv, z) + F(zv[8].z));
    zs = sw16_sum(zs, zs); zs += dpp<0x124>(zs); zs += dpp<0x128>(zs);
    if (cc < 4) *(GAS u16*)((GAS unsigned char*)out + oo) = (u16)cvtpk(hr + rbf(zs), 0.f);
    if (mi == 0 && lane == 0) __builtin_amdgcn_raw_buffer_store_b32(T, rws, 0, WS_E, AUX_SC1);
  }
}

extern "C" void kernel_launch(void* const* d_in, const int* in_sizes, int n_in, void* d_out, int out_size, void* d_ws, size_t ws_size, hipStream_t stream) {
  (void)hipFuncSetAttribute((const void*)fused, hipFuncAttributeMaxDynamicSharedMemorySize, L_END);
  fused<<<NWG, NTHR, L_END, stream>>>((const u16*)d_in[0], (const u16*)d_in[1], (const u16*)d_in[2], (const u16*)d_in[3], (const u16*)d_in[4],
      (const u16*)d_in[6], (const u16*)d_in[7], (const u16*)d_in[8], (u16*)d_out, (unsigned char*)d_ws);
}
```

```cpp
#include <hip/hip_runtime.h>
#include <hip/hip_bf16.h>
#include <stdint.h>

typedef unsigned short u16;
typedef unsigned long long u64;
typedef unsigned v4u __attribute__((ext_vector_type(4)));
typedef unsigned v2u __attribute__((ext_vector_type(2)));
typedef float f32x4 __attribute__((ext_vector_type(4)));
typedef float f32x2 __attribute__((ext_vector_type(2)));
typedef short s16x4 __attribute__((ext_vector_type(4)));
typedef __bf16 bf16x2 __attribute__((ext_vector_type(2)));
typedef __bf16 bf16x8 __attribute__((ext_vector_type(8)));
#define LAS __attribute__((address_space(3)))
#define GAS __attribute__((address_space(1)))

constexpr int D = 576, DH = 128, HD = 32 * DH;
constexpr int NWG = 256, NTHR = 576;
constexpr int NU = 128, UB = 16384;
constexpr unsigned LENS = 0x11113458u;
constexpr int lsum(int n) { return n == 0 ? 0 : lsum(n - 1) + (int)((LENS >> (4 * (n - 1))) & 15u); }
constexpr u64 STARTS = (u64)lsum(1) << 8 | (u64)lsum(2) << 16 | (u64)lsum(3) << 24 | (u64)lsum(4) << 32 | (u64)lsum(5) << 40 | (u64)lsum(6) << 48 | (u64)lsum(7) << 56;
constexpr int SSUM = NU - lsum(8), NREC = 12;
constexpr int SM = 25, SW = 27;
static_assert(2 * SM + 2 * SW == SSUM, "");
constexpr int NPU = 8;
constexpr int WTHR = 48, MTHR = 56;
constexpr unsigned WS_E = 0, WS_TK = 4096, WS_S1 = WS_TK + 8 * 256, S1_STR = 640, WS_S2 = WS_S1 + 64 * NREC * S1_STR, WS_SQ = WS_S2 + 8 * D * 8,
                   WS_S3 = WS_SQ + 64 * 512, WS_END = WS_S3 + 144 * D * 16;
constexpr unsigned L_W = 0, L_WD = 73728, L_WD2 = L_WD + 32 * 1056, L_WEND = L_WD2 + 4096;
constexpr unsigned L_QPART = 0, L_XW = 4096;
constexpr unsigned L_Q = L_WEND, L_IT = L_Q + 2048;
constexpr unsigned L_QP = L_IT + 16;
constexpr unsigned L_H = L_QP + 2 * 8 * 132 * 4;
constexpr unsigned L_RED = L_H + D * 4;
constexpr unsigned L_PRIV = L_RED + 320 * 48;
constexpr unsigned L_END = L_PRIV + 9 * 576;
constexpr int AUX_NT = 2, AUX_SC1 = 16, RS3 = 0x00020000, KV_BYTES = 128 << 20;
constexpr unsigned SPIN_CAP = 1u << 17;
constexpr float L2E = 1.4426950408889634f, SCALE = 0.08838834764831845f;

#define F(u) __uint_as_float(u)
#define U(f) __float_as_uint(f)
#define BAR() asm volatile("s_waitcnt lgkmcnt(0)\n\ts_barrier" ::: "memory")
#define RAWBAR() asm volatile("s_barrier" ::: "memory")
#define LWAIT() asm volatile("s_waitcnt lgkmcnt(0)" ::: "memory")
#define SB() __builtin_amdgcn_sched_barrier(0)
#define VM0() __builtin_amdgcn_s_waitcnt(0x0F70)

__device__ __forceinline__ float bflo(unsigned u) { return F(u << 16); }
__device__ __forceinline__ float bfhi(unsigned u) { return F(u & 0xFFFF0000u); }
__device__ __forceinline__ unsigned cvtpk(float lo, float hi) {
  const bf16x2 r = {(__bf16)lo, (__bf16)hi};
  return __builtin_bit_cast(unsigned, r);
}
__device__ __forceinline__ float rbf(float f) { return bflo(cvtpk(f, f)); }
__device__ __forceinline__ float dot2(unsigned a, unsigned b, float c) {
  return __builtin_amdgcn_fdot2_f32_bf16(__builtin_bit_cast(bf16x2, a), __builtin_bit_cast(bf16x2, b), c, false);
}
template <int CTRL> __device__ __forceinline__ float dpp(float x) {
  return __builtin_bit_cast(float, __builtin_amdgcn_mov_dpp(__builtin_bit_cast(int, x), CTRL, 0xf, 0xf, true));
}
__device__ __forceinline__ float vmax(float a, float b) { float r; asm("v_max_f32 %0, %1, %2" : "=v"(r) : "v"(a), "v"(b)); return r; }
#define DPPMAX(rot, x) asm("s_nop 1\n\tv_max_f32_dpp %0, %0, %0 row_ror:" #rot " row_mask:0xf bank_mask:0xf" : "+v"(x))
__device__ __forceinline__ float row_sum(float d) { d += dpp<0x128>(d); d += dpp<0x124>(d); d += dpp<0x122>(d); d += dpp<0x121>(d); return d; }
__device__ __forceinline__ void row_sum2(float& a, float& b) {
  asm("s_nop 2\n\t"
      "v_add_f32_dpp %0, %0, %0 row_ror:8 row_mask:0xf bank_mask:0xf\n\tv_add_f32_dpp %1, %1, %1 row_ror:8 row_mask:0xf bank_mask:0xf\n\ts_nop 0\n\t"
      "v_add_f32_dpp %0, %0, %0 row_ror:4 row_mask:0xf bank_mask:0xf\n\tv_add_f32_dpp %1, %1, %1 row_ror:4 row_mask:0xf bank_mask:0xf\n\ts_nop 0\n\t"
      "v_add_f32_dpp %0, %0, %0 row_ror:2 row_mask:0xf bank_mask:0xf\n\tv_add_f32_dpp %1, %1, %1 row_ror:2 row_mask:0xf bank_mask:0xf\n\ts_nop 0\n\t"
      "v_add_f32_dpp %0, %0, %0 row_ror:1 row_mask:0xf bank_mask:0xf\n\tv_add_f32_dpp %1, %1, %1 row_ror:1 row_mask:0xf bank_mask:0xf"
      : "+v"(a), "+v"(b));
}
__device__ __forceinline__ float sw32_sum(float a, float b) {
  auto t = __builtin_amdgcn_permlane32_swap(U(a), U(b), false, false);
  return F(t[0]) + F(t[1]);
}
__device__ __forceinline__ float sw16_sum(float a, float b) {
  auto t = __builtin_amdgcn_permlane16_swap(U(a), U(b), false, false);
  return F(t[0]) + F(t[1]);
}
__device__ __forceinline__ float xrow_sum(float x) { x = sw16_sum(x, x); return sw32_sum(x, x); }
__device__ __forceinline__ float xrow_max(float x) {
  auto s = __builtin_amdgcn_permlane16_swap(U(x), U(x), false, false);
  x = vmax(F(s[0]), F(s[1]));
  auto t = __builtin_amdgcn_permlane32_swap(U(x), U(x), false, false);
  return vmax(F(t[0]), F(t[1]));
}
__device__ __forceinline__ f32x4 mfma(v4u a, v4u b, f32x4 c) {
  return __builtin_amdgcn_mfma_f32_16x16x32_bf16(__builtin_bit_cast(bf16x8, a), __builtin_bit_cast(bf16x8, b), c, 0, 0, 0);
}
__device__ __forceinline__ v4u trfrag(LAS unsigned char* L, unsigned a0, unsigned a1) {
  const s16x4 x = __builtin_amdgcn_ds_read_tr16_b64_v4i16((LAS s16x4*)(L + a0));
  const s16x4 y = __builtin_amdgcn_ds_read_tr16_b64_v4i16((LAS s16x4*)(L + a1));
  const v2u p = __builtin_bit_cast(v2u, x), q = __builtin_bit_cast(v2u, y);
  return v4u{p.x, p.y, q.x, q.y};
}
__device__ __forceinline__ int kpos(int k) { return k < 16 ? 8 * (k >> 2) + (k & 3) : 8 * ((k - 16) >> 2) + 4 + (k & 3); }
__device__ __forceinline__ void ln_consts(float s1, float s2, float& mb, float& rd) {
  const float mean = s1 * (1.f / D), var = fmaxf(fmaf(-mean, mean, s2 * (1.f / D)), 0.f);
  mb = rbf(mean);
  rd = __builtin_amdgcn_rcpf(rbf(__builtin_amdgcn_sqrtf(rbf(rbf(var) + 1e-5f))));
}

__device__ __forceinline__ void st16(__amdgpu_buffer_rsrc_t rs, v4u d, unsigned off) {
  __builtin_amdgcn_raw_buffer_store_b128(d, rs, off, 0, AUX_SC1);
  asm volatile("s_nop 1" :: "v"(d));
}
#define BADM(v) (__builtin_amdgcn_ballot_w64((v).y != T) | __builtin_amdgcn_ballot_w64((v).w != T))
#define MBEG "s_nop 4\n\t"
#define ML(i, o) "s_mov_b64 exec, %[m" #i "]\n\tbuffer_load_dwordx4 %[v" #i "], %[o" #o "], %[rs], %[s" #i "] offen sc1\n\t"
#define MEND "s_mov_b64 exec, -1\n\ts_waitcnt vmcnt(0)"
#define POLL4(V, MK, OFF, S0, STR) asm volatile(MBEG ML(0, a) ML(1, a) ML(2, a) ML(3, a) MEND \
      : [v0] "+v"(V[0]), [v1] "+v"(V[1]), [v2] "+v"(V[2]), [v3] "+v"(V[3]) \
      : [m0] "s"(MK[0]), [m1] "s"(MK[1]), [m2] "s"(MK[2]), [m3] "s"(MK[3]), \
        [s0] "s"(S0), [s1] "s"(S0 + STR), [s2] "s"(S0 + 2 * STR), [s3] "s"(S0 + 3 * STR), [oa] "v"(OFF), [rs] "s"(rsw) : "memory")
#define SEL4(d, i, e) ((U(d[i].e) & m0) | (U(d[i + 1].e) & m1) | (U(d[i + 2].e) & m2) | (U(d[i + 3].e) & m3))

__device__ __forceinline__ float kdot(const v4u k, const v4u qv) { return dot2(k.w, qv.w, dot2(k.z, qv.z, dot2(k.y, qv.y, dot2(k.x, qv.x, 0.f)))); }
__device__ __forceinline__ void rest8(float d0, float d1, const v4u v0, const v4u v1, float& m, float& l, float (&acc)[8]) {
  row_sum2(d0, d1);
  unsigned p = cvtpk(d0, d1);
  p = cvtpk(bflo(p) * SCALE, bfhi(p) * SCALE);
  const float s0 = bflo(p) * L2E, s1 = bfhi(p) * L2E;
  const float mn = fmaxf(fmaxf(m, s0), s1);
  const float al = __builtin_amdgcn_exp2f(m - mn), p0 = __builtin_amdgcn_exp2f(s0 - mn), p1 = __builtin_amdgcn_exp2f(s1 - mn);
  m = mn; l = fmaf(l, al, p0 + p1);
#pragma unroll
  for (int j = 0; j < 4; ++j) {
    acc[2 * j] = fmaf(p1, bflo(v1[j]), fmaf(p0, bflo(v0[j]), acc[2 * j] * al));
    acc[2 * j + 1] = fmaf(p1, bfhi(v1[j]), fmaf(p0, bfhi(v0[j]), acc[2 * j + 1] * al));
  }
}
__device__ __forceinline__ void merge_wave(LAS unsigned char* L, unsigned rb, float m, float l, const float (&acc)[8], int w, int lane) {
  const float M = xrow_max(m), f = __builtin_amdgcn_exp2f(m - M);
  const float Lw = xrow_sum(l * f);
  const float z0 = sw32_sum(acc[0] * f, acc[4] * f), z1 = sw32_sum(acc[1] * f, acc[5] * f), z2 = sw32_sum(acc[2] * f, acc[6] * f), z3 = sw32_sum(acc[3] * f, acc[7] * f);
  *(LAS f32x2*)(L + rb + (w * 132 + 4 + 8 * (lane & 15) + 2 * (lane >> 4)) * 4) = f32x2{sw16_sum(z0, z2), sw16_sum(z1, z3)};
  if (lane == 0) *(LAS f32x2*)(L + rb + (w * 132) * 4) = f32x2{M, Lw};
}
__device__ __forceinline__ void publish(LAS unsigned char* L, unsigned rb, __amdgpu_buffer_rsrc_t rws, unsigned T, unsigned off, int lane) {
  const f32x2 ml = *(LAS f32x2*)(L + rb + ((lane & 7) * 132) * 4);
  f32x2 o[8];
#pragma unroll
  for (int i = 0; i < 8; ++i) o[i] = *(LAS f32x2*)(L + rb + (i * 132 + 4 + 2 * lane) * 4);
  float MM = ml.x;
  DPPMAX(4, MM); DPPMAX(2, MM); DPPMAX(1, MM);
  const float wt = __builtin_amdgcn_exp2f(ml.x - MM);
  float LL = wt * ml.y; LL += dpp<0x124>(LL); LL += dpp<0x122>(LL); LL += dpp<0x121>(LL);
  float o0 = dpp<0x150>(wt) * o[0].x, o1 = dpp<0x150>(wt) * o[0].y;
#define ADDW(i) o0 = fmaf(dpp<0x150 + i>(wt), o[i].x, o0); o1 = fmaf(dpp<0x150 + i>(wt), o[i].y, o1);
  ADDW(1) ADDW(2) ADDW(3) ADDW(4) ADDW(5) ADDW(6) ADDW(7)
  const float rl = __builtin_amdgcn_rcpf(LL);
  const unsigned pk = cvtpk(o0 * rl, o1 * rl), pn = (unsigned)__builtin_amdgcn_mov_dpp((int)pk, 0xB1, 0xf, 0xf, true), ls = U(MM + __builtin_amdgcn_logf(LL));
  st16(rws, v4u{(lane & 1) ? ls : pk, T, (lane & 1) ? ls : pn, T}, off);
}

__global__ void __launch_bounds__(NTHR, 3) fused(const u16* __restrict__ x, const u16* __restrict__ K, const u16* __restrict__ V, const u16* __restrict__ wq,
    const u16* __restrict__ wo, const u16* __restrict__ wg, const u16* __restrict__ wu, const u16* __restrict__ wd, u16* __restrict__ out, unsigned char* ws) {
  extern __shared__ __attribute__((aligned(16))) unsigned char smem[];
  LAS unsigned char* L = (LAS unsigned char*)smem;
  const f32x4 Z4 = {0.f, 0.f, 0.f, 0.f};
  const int tid = threadIdx.x, lane = tid & 63, wv = __builtin_amdgcn_readfirstlane(tid >> 6), r = lane >> 4, c = lane & 15, vo = lane * 16;
  const int g = blockIdx.x, x8 = g & 7, j = g >> 3, jg = j >> 2, h = x8 * 4 + (j >> 3), b = jg & 1, s = j & 3, G = b * 32 + h;
  const int pool = (jg + 2 * x8) & 7;
  const int grp = (jg + x8) & 7;
  const bool is_mlp = s < 2 || (s == 2 && grp < 2), is_wo = !is_mlp && (s == 2 || grp >= 5);
  const int cg = s == 2 ? grp - 2 : grp + 1, mi = x8 * 18 + (s < 2 ? 2 * grp + s : 16 + grp);
  const int t2 = grp < 2 ? SM : SW;
  const int slo = s < 3 ? s * SM : 2 * SM + t2, SS = (s < 2 ? SM : s == 2 ? t2 : SSUM - 2 * SM - t2) - NPU;
  const __amdgpu_buffer_rsrc_t rws = __builtin_amdgcn_make_buffer_rsrc((void*)ws, 0, (int)WS_END, RS3);
  const v4u rsw = {(unsigned)(size_t)ws, (unsigned)((size_t)ws >> 32) & 0xFFFFu, WS_END, (unsigned)RS3};
  unsigned T;
  if (wv == 0) {
    T = __builtin_amdgcn_readfirstlane(__builtin_amdgcn_raw_buffer_load_b32(rws, 0, WS_E, AUX_SC1)) + 1u;
    *(volatile LAS int*)(L + L_IT) = 0;
    *(LAS unsigned*)(L + L_IT + 4) = T;
    BAR();
    {
      RAWBAR(); RAWBAR(); RAWBAR();
      if (x8 == 0 && s == 0 && lane == 0) __hip_atomic_store((GAS u64*)(ws + WS_TK + pool * 256), (u64)T << 32, __ATOMIC_RELAXED, __HIP_MEMORY_SCOPE_AGENT);
      v4u pq[4];
      u64 mq[4];
      const unsigned oq = (pool * 8 + (lane >> 5)) * 512 + (lane & 31) * 16;
#pragma unroll
      for (int i = 0; i < 4; ++i) pq[i] = __builtin_amdgcn_raw_buffer_load_b128(rws, oq, WS_SQ + i * 1024, AUX_SC1);
      SB();
      RAWBAR();
#pragma nounroll
      for (unsigned spin = 0; spin < SPIN_CAP; ++spin) {
        u64 any = 0;
#pragma unroll
        for (int i = 0; i < 4; ++i) { mq[i] = BADM(pq[i]); any |= mq[i]; }
        if (any == 0) break;
        POLL4(pq, mq, oq, WS_SQ, 1024u);
      }
#pragma unroll
      for (int i = 0; i < 4; ++i) *(LAS v2u*)(L + L_Q + i * 512 + lane * 8) = v2u{pq[i].x, pq[i].z};
    }
    __builtin_amdgcn_s_setprio(3);
    const int sc = (lane & 7) ^ (r << 1);
    const unsigned char* src = is_wo ? (const unsigned char*)wo + ((size_t)(512 * x8 + (lane >> 3)) * D + 64 * cg) * 2 + 16 * sc
                                     : (const unsigned char*)(sc < 4 ? wg : wu) + (size_t)(lane >> 3) * 9216 + 64 * mi + 16 * (sc & 3);
    const int ph = is_wo ? 32 : is_mlp ? 36 : 0, k0 = 4 + (g * 37 >> 3) % (SS - 7), adv = is_wo ? 8 * 1152 : 8 * 9216;
    const unsigned char* sd = (const unsigned char*)wd + (unsigned)(32 * mi * 1152 + lane * 16);
    const unsigned char* sd2 = (const unsigned char*)wd + (unsigned)((32 * mi + (lane >> 3)) * 1152 + 1024 + 16 * sc);
    if (is_mlp) {
#pragma unroll
      for (int i = 0; i < 9; ++i) *(LAS unsigned*)(L + L_H + (64 * i + lane) * 4) = (unsigned)x[64 * i + lane] | ((unsigned)x[D + 64 * i + lane] << 16);
    }
    u64 tk = 0;
    const unsigned tko = WS_TK + pool * 256;
#define DRAW(inc) do { const u64 inc_ = (inc); asm volatile("s_nop 4\n\ts_mov_b64 exec, 1\n\tglobal_atomic_add_x2 %0, %1, %2, %3 sc0\n\ts_mov_b64 exec, -1" : "+v"(tk) : "v"(tko), "v"(inc_), "s"(ws) : "memory"); } while (0)
#define DRAWN() asm volatile("s_waitcnt vmcnt(0)" : "+v"(tk) :: "memory")
    int p = 0, seen = 0, due = SS - 1, cnt = 0;
#pragma nounroll
    for (int k = 4;; ++k) {
      if (k == due) {
        if (is_wo && cnt == 0) {
          DRAWN();
          seen = (unsigned)__builtin_amdgcn_readfirstlane((unsigned)(tk >> 32)) == T ? (int)__builtin_amdgcn_readfirstlane((unsigned)tk) : 64;
        }
        int cur = -1;
        if (seen < (is_wo ? WTHR : is_mlp ? MTHR : 64)) {
#pragma nounroll
          for (unsigned spin = 0; spin < SPIN_CAP; ++spin) {
            DRAW(1);
            DRAWN();
            if ((unsigned)__builtin_amdgcn_readfirstlane((unsigned)(tk >> 32)) == T) break;
          }
          const unsigned it = __builtin_amdgcn_readfirstlane((unsigned)tk);
          cur = ((unsigned)__builtin_amdgcn_readfirstlane((unsigned)(tk >> 32)) == T && it < 64) ? (int)it : -1;
        }
        ++cnt; seen = cur;
        *(volatile LAS int*)(L + L_IT) = cnt * 256 + cur + 1;
        if (cur >= 0) due += (LENS >> (4 * (cur >> 3))) & 15u;
      }
      if (is_wo && k == SS - 2) DRAW(0);
      const int pe = (k == k0 || k == k0 + 1) ? p + ph : p;
#pragma nounroll
      for (; p < pe; ++p) { __builtin_amdgcn_global_load_lds((const GAS void*)src, (LAS void*)(L + L_W + p * 1024), 16, 0, 0); src += adv; }
      if (k == SS - 1) VM0();
      BAR();
      if (k >= due) break;
    }
    if (!is_mlp) return;
    RAWBAR();
#pragma nounroll
    for (int q = 0; q < 32; ++q) { __builtin_amdgcn_global_load_lds((const GAS void*)sd, (LAS void*)(L + L_WD + q * 1056), 16, 0, 0); sd += 1152; }
#pragma unroll
    for (int e = 0; e < 4; ++e) __builtin_amdgcn_global_load_lds((const GAS void*)(sd2 + e * 8 * 1152), (LAS void*)(L + L_WD2 + e * 1024), 16, 0, 0);
  } else {
    const int w = wv - 1;
#define UOFF(G_, u_) (((G_) * NU + (u_)) * UB + w * 2048)
#define PIN() asm volatile("" : "+v"(m), "+v"(l), "+v"(acc[0]), "+v"(acc[1]), "+v"(acc[2]), "+v"(acc[3]), "+v"(acc[4]), "+v"(acc[5]), "+v"(acc[6]), "+v"(acc[7]) :: "memory")
    float m = -INFINITY, l = 0.f, acc[8];
#pragma unroll
    for (int i = 0; i < 8; ++i) acc[i] = 0.f;
    {
      v4u A[4], P[5][4];
      const __amdgpu_buffer_rsrc_t rk = __builtin_amdgcn_make_buffer_rsrc((void*)K, 0, KV_BYTES, RS3);
      const __amdgpu_buffer_rsrc_t rv = __builtin_amdgcn_make_buffer_rsrc((void*)V, 0, KV_BYTES, RS3);
#define LD1(X, e, u_) X[e] = __builtin_amdgcn_raw_buffer_load_b128((e) < 2 ? rk : rv, vo + ((e) & 1) * 1024, UOFF(G, slo + (u_)), AUX_NT)
#define LDP(X, u_) do { LD1(X, 0, u_); LD1(X, 1, u_); LD1(X, 2, u_); LD1(X, 3, u_); SB(); } while (0)
      v4u qv;
      {
        const __amdgpu_buffer_rsrc_t rq = __builtin_amdgcn_make_buffer_rsrc((void*)wq, 0, D * HD * 2, RS3);
        const __amdgpu_buffer_rsrc_t rx = __builtin_amdgcn_make_buffer_rsrc((void*)(x + b * D), 0, D * 2, RS3);
        const int qo = (18 * r * HD + 8 * c) * 2, qs = (72 * w * HD + h * DH) * 2;
        v4u wr[18];
#define LDW(i) wr[i] = __builtin_amdgcn_raw_buffer_load_b128(rq, qo, qs + (i) * HD * 2, 0)
        __builtin_amdgcn_s_setprio(3);
        LD1(A, 0, 0); LD1(A, 2, 0); SB();
        const v4u xa = __builtin_amdgcn_raw_buffer_load_b128(rx, vo, 0, 0), xb = __builtin_amdgcn_raw_buffer_load_b128(rx, vo + 1024, 0, 0);
        SB();
        LDW(0); LDW(1); LD1(A, 1, 0); LDW(2); LDW(3); LD1(A, 3, 0); SB();
#pragma unroll
        for (int i = 0; i < 4; ++i) { LDW(4 + i); LD1(P[0], i, 1); SB(); }
        const unsigned xl = L_XW + w * 2048;
        *(LAS v4u*)(L + xl + vo) = xa; *(LAS v4u*)(L + xl + 1024 + vo) = xb;
        unsigned xr[9];
#pragma unroll
        for (int p = 0; p < 9; ++p) xr[p] = *(LAS unsigned*)(L + xl + (72 * w + 18 * r) * 2 + 4 * p);
        float s1 = 0.f, s2 = 0.f;
#pragma unroll
        for (int i = 0; i < 4; ++i) { s1 = dot2(xb[i], 0x3F803F80u, dot2(xa[i], 0x3F803F80u, s1)); s2 = dot2(xb[i], xb[i], dot2(xa[i], xa[i], s2)); }
        float mb, rd;
        ln_consts(xrow_sum(row_sum(s1)), xrow_sum(row_sum(s2)), mb, rd);
        unsigned lp[9];
#pragma unroll
        for (int p = 0; p < 9; ++p) { const unsigned t = cvtpk(bflo(xr[p]) - mb, bfhi(xr[p]) - mb); lp[p] = cvtpk(bflo(t) * rd, bfhi(t) * rd); }
        float qa[8];
#pragma unroll
        for (int i = 0; i < 8; ++i) qa[i] = 0.f;
        SB();
#pragma unroll
        for (int p = 0; p < 9; ++p) {
#pragma unroll
          for (int d = 0; d < 4; ++d) {
            qa[2 * d] = dot2(__builtin_amdgcn_perm(wr[2 * p + 1][d], wr[2 * p][d], 0x05040100u), lp[p], qa[2 * d]);
            qa[2 * d + 1] = dot2(__builtin_amdgcn_perm(wr[2 * p + 1][d], wr[2 * p][d], 0x07060302u), lp[p], qa[2 * d + 1]);
          }
          SB();
          if (p == 1) {
#pragma unroll
            for (int i = 0; i < 10; ++i) { LDW(8 + i); if (i < 8) LD1(P[1 + (i >> 2)], i & 3, 2 + (i >> 2)); SB(); }
            __builtin_amdgcn_s_setprio(0);
          }
        }
        LDP(P[3], 4); LDP(P[4], 5);
        const float z0 = sw32_sum(qa[0], qa[4]), z1 = sw32_sum(qa[1], qa[5]), z2 = sw32_sum(qa[2], qa[6]), z3 = sw32_sum(qa[3], qa[7]);
        *(LAS f32x2*)(L + L_QPART + (w * 128 + 8 * c + 2 * r) * 4) = f32x2{sw16_sum(z0, z2), sw16_sum(z1, z3)};
        BAR();
        const f32x4 q0 = *(LAS f32x4*)(L + L_QPART + (r * 256 + 8 * c) * 4) + *(LAS f32x4*)(L + L_QPART + (r * 256 + 128 + 8 * c) * 4);
        const f32x4 q1 = *(LAS f32x4*)(L + L_QPART + (r * 256 + 8 * c + 4) * 4) + *(LAS f32x4*)(L + L_QPART + (r * 256 + 128 + 8 * c + 4) * 4);
        const float y0 = sw32_sum(q0.x, q1.x), y1 = sw32_sum(q0.y, q1.y), y2 = sw32_sum(q0.z, q1.z), y3 = sw32_sum(q0.w, q1.w);
        const unsigned pk = cvtpk(sw16_sum(y0, y2), sw16_sum(y1, y3));
        auto t16 = __builtin_amdgcn_permlane16_swap(pk, pk, false, false);
        auto te = __builtin_amdgcn_permlane32_swap(t16[0], t16[0], false, false), to = __builtin_amdgcn_permlane32_swap(t16[1], t16[1], false, false);
        qv = v4u{te[0], to[0], te[1], to[1]};
        T = __builtin_amdgcn_readfirstlane(*(LAS unsigned*)(L + L_IT + 4));
      }
#define USE(X) do { rest8(kdot(X[0], qv), kdot(X[1], qv), X[2], X[3], m, l, acc); PIN(); SB(); } while (0)
      USE(A);
      LDP(A, 6);
      if (s == 0 && w == 0 && r < 2) st16(rws, v4u{r ? qv.z : qv.x, T, r ? qv.w : qv.y, T}, WS_SQ + ((pool * 8 + x8) * 32 + 2 * c + r) * 16);
      USE(P[0]); USE(P[1]);
      LDP(P[0], 7);
      USE(P[2]); USE(P[3]); USE(P[4]);
      USE(A);
      LDP(A, 8);
      USE(P[0]);
      int rec = (h * 2 + b) * NREC + s, rem = SS, cnt = 0, Gc = G, uc = slo + NPU;
      bool live = true;
#pragma nounroll
      do {
        const bool fin = rem == 1;
        const unsigned rb = L_QP + (cnt & 1) * 4224, ro = WS_S1 + rec * S1_STR + ((lane & 1) ? 512 : lane * 8);
        float d0 = kdot(A[0], qv), d1 = kdot(A[1], qv);
        asm volatile("" : "+v"(d0), "+v"(d1) :: "memory");
        SB();
        --rem; ++uc;
        {
          const __amdgpu_buffer_rsrc_t rk_ = __builtin_amdgcn_make_buffer_rsrc((void*)K, 0, fin ? 0 : KV_BYTES, RS3);
          const int so = fin ? 0 : UOFF(Gc, uc);
          SB();
          A[0] = __builtin_amdgcn_raw_buffer_load_b128(rk_, vo, so, AUX_NT); A[1] = __builtin_amdgcn_raw_buffer_load_b128(rk_, vo + 1024, so, AUX_NT);
          SB();
        }
        rest8(d0, d1, A[2], A[3], m, l, acc);
        PIN(); SB();
        if (fin) {
          ++cnt;
          int wd_;
#pragma nounroll
          for (unsigned spin = 0; spin < (SPIN_CAP << 4); ++spin) {
            wd_ = __builtin_amdgcn_readfirstlane(*(volatile LAS int*)(L + L_IT));
            if ((wd_ >> 8) == cnt) break;
          }
          const int it = (wd_ >> 8) == cnt ? (wd_ & 255) - 1 : -1;
          live = it >= 0;
          const int itx = it & 7, h2 = itx * 4 + (((pool - 2 * itx) & 7) >> 1), pc = (it >> 3) & 7;
          Gc = b * 32 + h2; uc = SSUM + (int)((STARTS >> (8 * pc)) & 255u); rem = (LENS >> (4 * pc)) & 15u;
          rec = (h2 * 2 + b) * NREC + 4 + pc;
          qv = *(LAS v4u*)(L + L_Q + itx * 256 + c * 16);
          const v4u rk4 = {(unsigned)(size_t)K, (unsigned)((size_t)K >> 32) & 0xFFFFu, live ? (unsigned)KV_BYTES : 0u, (unsigned)RS3};
          const int so = live ? UOFF(Gc, uc) : 0;
          asm volatile("s_nop 4\n\tbuffer_load_dwordx4 %0, %2, %3, %4 offen nt\n\tbuffer_load_dwordx4 %1, %2, %3, %4 offen offset:1024 nt"
                       : "+v"(A[0]), "+v"(A[1]) : "v"(vo), "s"(rk4), "s"(so) : "memory");
          SB();
        }
        {
          const __amdgpu_buffer_rsrc_t rv_ = __builtin_amdgcn_make_buffer_rsrc((void*)V, 0, live ? KV_BYTES : 0, RS3);
          const int so = live ? UOFF(Gc, uc) : 0;
          SB();
          A[2] = __builtin_amdgcn_raw_buffer_load_b128(rv_, vo, so, AUX_NT); A[3] = __builtin_amdgcn_raw_buffer_load_b128(rv_, vo + 1024, so, AUX_NT);
          SB();
        }
        if (fin) {
          merge_wave(L, rb, m, l, acc, w, lane);
          m = -INFINITY; l = 0.f;
#pragma unroll
          for (int i = 0; i < 8; ++i) acc[i] = 0.f;
        }
        BAR();
        if (fin && w == 0) publish(L, rb, rws, T, ro, lane);
      } while (live);
      asm volatile("s_waitcnt vmcnt(2)" : "+v"(A[0]), "+v"(A[1]) :: "memory");
    }
    if (!is_mlp) {
      if (is_wo) {
        const int q4 = c >> 2, p4 = c & 3, fr = ((2 * r + (q4 >> 1)) & 3) << 1;
        v4u bf[2][4];
#pragma unroll
        for (int ks = 0; ks < 2; ++ks)
#pragma unroll
          for (int t = 0; t < 4; ++t) {
            const unsigned a = L_W + (64 * w + 32 * ks + 4 * r + q4) * 128 + (((2 * t + (p4 >> 1)) ^ fr) * 16) + 8 * (p4 & 1);
            bf[ks][t] = trfrag(L, a, a + 16 * 128);
          }
        const int pb = lane >> 5, k2 = 4 * c + 2 * (r & 1);
        const unsigned ob = (((4 * x8 + (w >> 1)) * 2 + pb) * NREC + 6 * (r & 1)) * S1_STR;
        unsigned o1 = ob + (16 * (w & 1) + c) * 16, o2 = ob + ((c & 7) < 6 ? (c & 7) : 5) * S1_STR + 512, m6 = (c & 7) < 6 ? ~0u : 0u;
        unsigned aw = L_PRIV + w * 576 + pb * 128 + (k2 & 32) * 2 + kpos(k2 & 31) * 2, ar = L_PRIV + w * 576 + (c & 1) * 128 + r * 16, ad = L_RED + (w * 64 + lane) * 8;
        unsigned m0 = r == 0 ? ~0u : 0u, m1 = r == 1 ? ~0u : 0u, m2 = r == 2 ? ~0u : 0u, m3 = r == 3 ? ~0u : 0u;
        asm volatile("" : "+v"(o1), "+v"(o2), "+v"(m6), "+v"(aw), "+v"(ar), "+v"(ad), "+v"(m0), "+v"(m1), "+v"(m2), "+v"(m3));
#pragma unroll
        for (int t = 0; t < 4; ++t) asm volatile("" : "+v"(bf[0][t]), "+v"(bf[1][t]));
        v4u ov[7];
        u64 mk[7];
#pragma unroll
        for (int i = 0; i < 7; ++i) mk[i] = ~0ull;
#pragma nounroll
        for (unsigned spin = 0; spin < SPIN_CAP; ++spin) {
          asm volatile(MBEG ML(6, b) ML(0, a) ML(1, a) ML(2, a) ML(3, a) ML(4, a) ML(5, a) MEND
              : [v0] "+v"(ov[0]), [v1] "+v"(ov[1]), [v2] "+v"(ov[2]), [v3] "+v"(ov[3]), [v4] "+v"(ov[4]), [v5] "+v"(ov[5]), [v6] "+v"(ov[6])
              : [m0] "s"(mk[0]), [m1] "s"(mk[1]), [m2] "s"(mk[2]), [m3] "s"(mk[3]), [m4] "s"(mk[4]), [m5] "s"(mk[5]), [m6] "s"(mk[6]),
                [s0] "s"(WS_S1), [s1] "s"(WS_S1 + S1_STR), [s2] "s"(WS_S1 + 2 * S1_STR), [s3] "s"(WS_S1 + 3 * S1_STR), [s4] "s"(WS_S1 + 4 * S1_STR), [s5] "s"(WS_S1 + 5 * S1_STR),
                [s6] "s"(WS_S1), [oa] "v"(o1), [ob] "v"(o2), [rs] "s"(rsw) : "memory");
          u64 any = 0;
#pragma unroll
          for (int i = 0; i < 7; ++i) { mk[i] = BADM(ov[i]); any |= mk[i]; }
          if (any == 0) break;
        }
        float Mx = F(ov[6].x);
        DPPMAX(4, Mx); DPPMAX(2, Mx); DPPMAX(1, Mx);
        { auto t = __builtin_amdgcn_permlane16_swap(U(Mx), U(Mx), false, false); Mx = vmax(F(t[0]), F(t[1])); }
        const float wt = __builtin_amdgcn_exp2f(F(ov[6].x) - Mx);
        float LL = F(U(wt) & m6);
        LL += dpp<0x124>(LL); LL += dpp<0x122>(LL); LL += dpp<0x121>(LL);
        LL = sw16_sum(LL, LL);
        float e0, e1, e2, e3;
        { const float wi = dpp<0x150>(wt); e0 = wi * bflo(ov[0].x); e1 = wi * bfhi(ov[0].x); e2 = wi * bflo(ov[0].z); e3 = wi * bfhi(ov[0].z); }
#define ADDREC(i) { const float wi = dpp<0x150 + i>(wt); e0 = fmaf(wi, bflo(ov[i].x), e0); e1 = fmaf(wi, bfhi(ov[i].x), e1); e2 = fmaf(wi, bflo(ov[i].z), e2); e3 = fmaf(wi, bfhi(ov[i].z), e3); }
        ADDREC(1) ADDREC(2) ADDREC(3) ADDREC(4) ADDREC(5)
        const float rl = __builtin_amdgcn_rcpf(LL);
        *(LAS unsigned*)(L + aw) = cvtpk(sw16_sum(e0, e2) * rl, sw16_sum(e1, e3) * rl);
        LWAIT();
        const v4u a0 = *(LAS v4u*)(L + ar), a1 = *(LAS v4u*)(L + ar + 64);
        f32x4 d[4];
#pragma unroll
        for (int t = 0; t < 4; ++t) d[t] = mfma(a0, bf[0][t], Z4);
#pragma unroll
        for (int t = 0; t < 4; ++t) d[t] = mfma(a1, bf[1][t], d[t]);
        *(LAS v2u*)(L + ad) = v2u{SEL4(d, 0, x), SEL4(d, 0, y)};
        BAR();
        if (w == 0) {
          f32x2 y[8];
#pragma unroll
          for (int i = 0; i < 8; ++i) y[i] = *(LAS f32x2*)(L + L_RED + (i * 64 + lane) * 8);
          const f32x2 yy = ((y[0] + y[1]) + (y[2] + y[3])) + ((y[4] + y[5]) + (y[6] + y[7]));
          const unsigned pk = cvtpk(yy.x, yy.y), pn = (unsigned)__builtin_amdgcn_mov_dpp((int)pk, 0xB1, 0xf, 0xf, true);
          if (!(lane & 1)) st16(rws, v4u{pk, T, pn, T}, lane * 8 + WS_S2 + (x8 * D + 64 * cg) * 8);
        }
      }
      return;
    }
    RAWBAR();
  }
  const int w = wv;
  const int q4 = c >> 2, p4 = c & 3, pb = lane >> 5, cc = lane & 31, fr = ((2 * r + (q4 >> 1)) & 3) << 1;
  const unsigned pv = L_PRIV + w * 576;
  *(LAS u16*)(L + pv + 288 + lane * 2) = (u16)0x3F80;
  const int kh = 2 * cc + pb;
  unsigned o2 = pb * D * 8 + (32 * w + cc) * 16;
  v4u yv[4];
  u64 mk[9];
#pragma unroll
  for (int i = 0; i < 9; ++i) mk[i] = ~0ull;
  const unsigned ig = ((cc >> 4) * 64 + (cc & 15)) * 48;
  unsigned i1 = L_RED + pb * 768 + ig, i3 = L_RED + 2 * 768 + ig + pb * 6144, i4 = L_RED + (256 + pb * 16 + ((cc & 1) ? 4 * pb : 8)) * 48;
  unsigned aA = pv + (c < 4 ? 0 : c < 8 ? 144 : 288) + r * 16, ah = pv + (kh >> 5) * 64 + kpos(kh & 31) * 2, aa = pv + 432 + pb * 64 + kpos(cc) * 2,
           aH = L_H + (64 * w + kh) * 4, aR = L_RED + lane * 48 + w * 4, aB = pv + 432 + (c & 1) * 64 + r * 16;
  unsigned m0 = r == 0 ? ~0u : 0u, m1 = r == 1 ? ~0u : 0u, m2 = r == 2 ? ~0u : 0u, m3 = r == 3 ? ~0u : 0u;
  unsigned ag = L_W + (64 * w + 4 * r + q4) * 128 + 8 * (p4 & 1), ad0 = L_WD + (4 * r + q4) * 1056 + 288 * (w & 3) + 8 * p4, ae0 = L_WD2 + (4 * r + q4) * 128 + 8 * (p4 & 1);
  unsigned g0 = ag + (((p4 >> 1) ^ fr) * 16), g1 = ag + (((2 + (p4 >> 1)) ^ fr) * 16), g2 = ag + (((4 + (p4 >> 1)) ^ fr) * 16), g3 = ag + (((6 + (p4 >> 1)) ^ fr) * 16);
  unsigned s3a = WS_S3 + (mi * D + 144 * w) * 16 + vo, s3b = WS_S3 + (mi * D + 144 * w + 128 + c) * 16;
  asm volatile("" : "+v"(i1), "+v"(i3), "+v"(i4), "+v"(aA), "+v"(ah), "+v"(aa), "+v"(aH), "+v"(aR), "+v"(aB), "+v"(m0), "+v"(m1), "+v"(m2), "+v"(m3));
  asm volatile("" : "+v"(g0), "+v"(g1), "+v"(g2), "+v"(g3), "+v"(ad0), "+v"(s3a), "+v"(s3b));
  unsigned xw = *(LAS unsigned*)(L + aH);
  asm volatile("" : "+v"(xw));
  VM0();
  RAWBAR();
  v4u gf[2][4], df[9];
#pragma unroll
  for (int ks = 0; ks < 2; ++ks) {
    gf[ks][0] = trfrag(L, g0 + ks * 4096, g0 + ks * 4096 + 2048); gf[ks][1] = trfrag(L, g1 + ks * 4096, g1 + ks * 4096 + 2048);
    gf[ks][2] = trfrag(L, g2 + ks * 4096, g2 + ks * 4096 + 2048); gf[ks][3] = trfrag(L, g3 + ks * 4096, g3 + ks * 4096 + 2048);
  }
  if (w < 4) {
#pragma unroll
    for (int i = 0; i < 9; ++i) {
      const bool hi = i >= 5 && w == 3;
      const unsigned a = hi ? ae0 + ((2 * (i - 5) + (p4 >> 1)) ^ fr) * 16 : ad0 + i * 32;
      df[i] = trfrag(L, a, a + (hi ? 16 * 128 : 16 * 1056));
    }
  }
#pragma unroll
  for (int t = 0; t < 4; ++t) asm volatile("" : "+v"(gf[0][t]), "+v"(gf[1][t]));
#pragma unroll
  for (int t = 0; t < 9; ++t) asm volatile("" : "+v"(df[t]));
#pragma nounroll
  for (unsigned spin = 0; spin < SPIN_CAP; ++spin) {
    POLL4(yv, mk, o2, WS_S2, 2 * D * 8);
#pragma unroll
    for (int i = 0; i < 4; ++i) mk[i] = BADM(yv[i]);
    if ((mk[0] | mk[1] | mk[2] | mk[3]) == 0) break;
    if ((mk[0] & mk[1] & mk[2] & mk[3]) != 0) __builtin_amdgcn_s_sleep(4);
  }
  {
    const float y0 = sw32_sum((bflo(yv[0].x) + bflo(yv[1].x)) + (bflo(yv[2].x) + bflo(yv[3].x)), (bflo(yv[0].z) + bflo(yv[1].z)) + (bflo(yv[2].z) + bflo(yv[3].z)));
    const float y1 = sw32_sum((bfhi(yv[0].x) + bfhi(yv[1].x)) + (bfhi(yv[2].x) + bfhi(yv[3].x)), (bfhi(yv[0].z) + bfhi(yv[1].z)) + (bfhi(yv[2].z) + bfhi(yv[3].z)));
    const unsigned yb = cvtpk(y0, y1), hb = cvtpk(bflo(xw) + bflo(yb), bfhi(xw) + bfhi(yb));
    *(LAS u16*)(L + ah) = (u16)hb;
    *(LAS u16*)(L + ah + 144) = (u16)(hb >> 16);
    *(LAS unsigned*)(L + aH) = hb;
    LWAIT();
    const v4u a0 = *(LAS v4u*)(L + aA), a1 = *(LAS v4u*)(L + aA + 64);
    f32x4 d[5];
#pragma unroll
    for (int t = 0; t < 4; ++t) d[t] = mfma(a0, gf[0][t], Z4);
    d[4] = mfma(a0, a0, Z4);
#pragma unroll
    for (int t = 0; t < 4; ++t) d[t] = mfma(a1, gf[1][t], d[t]);
    d[4] = mfma(a1, a1, d[4]);
#pragma unroll
    for (int t = 0; t < 5; ++t) *(LAS float*)(L + aR + t * 3072) = d[t].x;
  }
  BAR();
  if (w < 4) {
#define SUM9(a) ({ const f32x4 t_ = *(LAS f32x4*)(L + (a)) + *(LAS f32x4*)(L + (a) + 16); ((t_.x + t_.y) + (t_.z + t_.w)) + *(LAS float*)(L + (a) + 32); })
    const float St = SUM9(i4), Gs = SUM9(i1), Us = SUM9(i1 + 6144), Cs = SUM9(i3);
    auto cs = __builtin_amdgcn_permlane32_swap(U(Cs), U(Cs), false, false);
    float mb, rd;
    ln_consts(dpp<0xA0>(St), dpp<0xF5>(St), mb, rd);
    const unsigned gu = cvtpk(fmaf(-mb, F(cs[0]), Gs) * rd, fmaf(-mb, F(cs[1]), Us) * rd);
    const float gg = bflo(gu);
    const float kk = gg * fmaf(gg * gg, -2.f * L2E * 0.7978845608028654f * 0.044715f, -2.f * L2E * 0.7978845608028654f);
    const float ge = gg * __builtin_amdgcn_rcpf(1.f + __builtin_amdgcn_exp2f(kk));
    const float act = rbf(ge) * bfhi(gu);
    *(LAS u16*)(L + aa) = (u16)cvtpk(act, act);
    LWAIT();
    const v4u a = *(LAS v4u*)(L + aB);
    f32x4 d[9];
#pragma unroll
    for (int i = 0; i < 9; ++i) d[i] = mfma(a, df[i], Z4);
    st16(rws, v4u{SEL4(d, 0, x), T, SEL4(d, 0, y), T}, s3a);
    st16(rws, v4u{SEL4(d, 4, x), T, SEL4(d, 4, y), T}, s3a + 1024);
    st16(rws, v4u{U(d[8].x), T, U(d[8].y), T}, s3b);
  }
  if (w == 4) {
    const int n = 4 * mi + (lane & 3);
    const unsigned hres = *(LAS unsigned*)(L + L_H + n * 4);
    float hr = pb ? bfhi(hres) : bflo(hres);
    unsigned o3 = ((lane >> 2) * D + n) * 16, oo = (pb * D + n) * 2;
    asm volatile("" : "+v"(hr), "+v"(o3), "+v"(oo));
    v4u zv[9];
#pragma unroll
    for (int i = 0; i < 9; ++i) mk[i] = ~0ull;
    constexpr unsigned S3S = 16 * D * 16;
    __builtin_amdgcn_s_sleep(27);
#pragma nounroll
    for (unsigned spin = 0; spin < SPIN_CAP; ++spin) {
      asm volatile(MBEG ML(0, a) ML(1, a) ML(2, a) ML(3, a) ML(4, a) ML(5, a) ML(6, a) ML(7, a) ML(8, a) MEND
          : [v0] "+v"(zv[0]), [v1] "+v"(zv[1]), [v2] "+v"(zv[2]), [v3] "+v"(zv[3]), [v4] "+v"(zv[4]), [v5] "+v"(zv[5]), [v6] "+v"(zv[6]), [v7] "+v"(zv[7]), [v8] "+v"(zv[8])
          : [m0] "s"(mk[0]), [m1] "s"(mk[1]), [m2] "s"(mk[2]), [m3] "s"(mk[3]), [m4] "s"(mk[4]), [m5] "s"(mk[5]), [m6] "s"(mk[6]), [m7] "s"(mk[7]), [m8] "s"(mk[8]),
            [s0] "s"(WS_S3), [s1] "s"(WS_S3 + S3S), [s2] "s"(WS_S3 + 2 * S3S), [s3] "s"(WS_S3 + 3 * S3S), [s4] "s"(WS_S3 + 4 * S3S), [s5] "s"(WS_S3 + 5 * S3S),
            [s6] "s"(WS_S3 + 6 * S3S), [s7] "s"(WS_S3 + 7 * S3S), [s8] "s"(WS_S3 + 8 * S3S),
            [oa] "v"(o3), [rs] "s"(rsw) : "memory");
      u64 any = 0;
#pragma unroll
      for (int i = 0; i < 9; ++i) { mk[i] = BADM(zv[i]); any |= mk[i]; }
      if (any == 0) break;
      __builtin_amdgcn_s_sleep(4);
    }
#define SUM8(V, e) (((F(V[0].e) + F(V[1].e)) + (F(V[2].e) + F(V[3].e))) + ((F(V[4].e) + F(V[5].e)) + (F(V[6].e) + F(V[7].e))))
    float zs = sw32_sum(SUM8(zv, x) + F(zv[8].x), SUM8(zv, z) + F(zv[8].z));
    zs = sw16_sum(zs, zs); zs += dpp<0x124>(zs); zs += dpp<0x128>(zs);
    if (cc < 4) *(GAS u16*)((GAS unsigned char*)out + oo) = (u16)cvtpk(hr + rbf(zs), 0.f);
    if (mi == 0 && lane == 0) __builtin_amdgcn_raw_buffer_store_b32(T, rws, 0, WS_E, AUX_SC1);
  }
}

extern "C" void kernel_launch(void* const* d_in, const int* in_sizes, int n_in, void* d_out, int out_size, void* d_ws, size_t ws_size, hipStream_t stream) {
  (void)hipFuncSetAttribute((const void*)fused, hipFuncAttributeMaxDynamicSharedMemorySize, L_END);
  fused<<<NWG, NTHR, L_END, stream>>>((const u16*)d_in[0], (const u16*)d_in[1], (const u16*)d_in[2], (const u16*)d_in[3], (const u16*)d_in[4],
      (const u16*)d_in[6], (const u16*)d_in[7], (const u16*)d_in[8], (u16*)d_out, (unsigned char*)d_ws);
}
```
